```python
import torch

def _softmax(x, axis=-1):
    return torch.softmax(x, dim=axis)
def _gelu(x):
    return torch.nn.functional.gelu(x, approximate="tanh")
def _silu(x):
    return torch.nn.functional.silu(x)
def _sigmoid(x):
    return torch.sigmoid(x)
def _layernorm(x, axis=-1, eps=1e-5):
    m = x.mean(dim=axis, keepdim=True)
    v = x.var(dim=axis, keepdim=True, unbiased=False)
    return (x - m) / torch.sqrt(v + eps)
def _scatter_add(tgt, idx, src, axis=0):
    return tgt.clone().index_add_(axis, idx, src)
def _segment_sum(src, seg, n):
    out = torch.zeros((n,) + src.shape[1:], dtype=src.dtype)
    return out.index_add_(0, seg, src)
def _pad(x, pw):
    flat = tuple(v for pair in reversed(pw) for v in pair)
    return torch.nn.functional.pad(x, flat)

kernel_name = 'parallel_sum__gqa_block_cached__geglu_ffn__decode'

def setup_inputs():

    _w = (lambda a: (lambda t: t.to(torch.bfloat16) if t.is_floating_point() else t)(torch.as_tensor(a)))
    def _A_setup():
        B, Sq, D = 1, 1, 1024
        Hq, Hkv, Sk, Dh = 32, 4, 65536, 128
        assert Hq % Hkv == 0, (Hq, Hkv)
        return {
            "x": _w((lambda *s: torch.randn(*s, dtype=torch.bfloat16) * 0.05)(B, Sq, D)),
            "k_cache": _w((lambda *s: torch.randn(*s, dtype=torch.bfloat16) * 3.2)(B, Hkv, Sk, Dh)),
            "v_cache": _w((lambda *s: torch.randn(*s, dtype=torch.bfloat16) * 1.6)(B, Hkv, Sk, Dh)),
            "wq": _w((lambda *s: torch.randn(*s, dtype=torch.bfloat16) * 3.2)(D, Hq * Dh) * (D ** -0.5)),
            "wo": _w((lambda *s: torch.randn(*s, dtype=torch.bfloat16) * 1.6)(Hq * Dh, D) * ((Hq * Dh) ** -0.5)),
        }
    _A_in = _A_setup()

    def _B_setup():
        B, S, D, F = 1, 1, 1024, 3584
        return {
            "x": _w((lambda *s: torch.randn(*s, dtype=torch.bfloat16) * 0.05)(B, S, D)),
            "w_gate": _w((lambda *s: torch.randn(*s, dtype=torch.bfloat16) * 0.05)(D, F)),
            "w_up": _w((lambda *s: torch.randn(*s, dtype=torch.bfloat16) * 0.05)(D, F)),
            "w_down": _w((lambda *s: torch.randn(*s, dtype=torch.bfloat16) * 0.05)(F, D)),
        }
    _B_in = _B_setup()

    return {
        "A_x": _A_in["x"],
        "A_k_cache": _A_in["k_cache"],
        "A_v_cache": _A_in["v_cache"],
        "A_wq": _A_in["wq"],
        "A_wo": _A_in["wo"],
        "B_x": _B_in["x"],
        "B_w_gate": _B_in["w_gate"],
        "B_w_up": _B_in["w_up"],
        "B_w_down": _B_in["w_down"],
    }

def reference(A_x, A_k_cache, A_v_cache, A_wq, A_wo, B_x, B_w_gate, B_w_up, B_w_down):
    def _A_ref(x, k_cache, v_cache, wq, wo):
        B, Sq, D = x.shape
        Hq, Dh = 32, 128
        Hkv = k_cache.shape[1]
        rep = Hq // Hkv
        q = (lambda x, *d: x.permute(*d))((x @ wq).reshape(B, Sq, Hq, Dh), 0, 2, 1, 3)
        k = (lambda x, n, axis: torch.repeat_interleave(x, n, dim=axis))(k_cache, rep, axis=1)
        v = (lambda x, n, axis: torch.repeat_interleave(x, n, dim=axis))(v_cache, rep, axis=1)
        scores = torch.einsum("bhqd,bhkd->bhqk", q, k) * (Dh ** -0.5)
        p = _softmax(scores, axis=-1)
        o = torch.einsum("bhqk,bhkd->bhqd", p, v)
        return (lambda x, *d: x.permute(*d))(o, 0, 2, 1, 3).reshape(B, Sq, Hq * Dh) @ wo

    def _B_ref(x, w_gate, w_up, w_down):
        return (_gelu(x @ w_gate) * (x @ w_up)) @ w_down

    x = A_x
    xn = _layernorm(x, axis=-1)
    return x + _A_ref(xn, A_k_cache, A_v_cache, A_wq, A_wo) + _B_ref(xn, B_w_gate, B_w_up, B_w_down)
```

```cpp
#include <hip/hip_runtime.h>
#include <hip/hip_bf16.h>
#include <stdint.h>

#define GAS __attribute__((address_space(1)))
typedef unsigned v4u __attribute__((ext_vector_type(4)));
typedef unsigned v2u __attribute__((ext_vector_type(2)));
typedef float f32x4 __attribute__((ext_vector_type(4)));
typedef short bf16x8 __attribute__((ext_vector_type(8)));

constexpr int D = 1024, HQ = 32, HKV = 4, SK = 65536, DH = 128, FF = 3584;
constexpr int NWG = 256, NTHR = 512;

__device__ __forceinline__ float bf_lo(unsigned u) { return __builtin_bit_cast(float, u << 16); }
__device__ __forceinline__ float bf_hi(unsigned u) { return __builtin_bit_cast(float, u & 0xffff0000u); }
typedef __bf16 bf16x2_t __attribute__((ext_vector_type(2)));
typedef float f32x2 __attribute__((ext_vector_type(2)));
__device__ __forceinline__ unsigned cvtpk(float lo, float hi) {
  return __builtin_bit_cast(unsigned, __builtin_convertvector(f32x2{lo, hi}, bf16x2_t));
}
__device__ __forceinline__ v4u ldnt(const v4u* p) { return __builtin_nontemporal_load(p); }
__device__ __forceinline__ f32x4 mfma(v4u a, v4u b, f32x4 c) {
  return __builtin_amdgcn_mfma_f32_16x16x32_bf16(__builtin_bit_cast(bf16x8, a), __builtin_bit_cast(bf16x8, b), c, 0, 0, 0);
}
template <int CTRL> __device__ __forceinline__ float dpp(float x) {
  return __builtin_bit_cast(float, __builtin_amdgcn_mov_dpp(__builtin_bit_cast(int, x), CTRL, 0xf, 0xf, true));
}
constexpr int XOR1 = 0xB1, XOR2 = 0x4E, XOR7 = 0x141, XOR8 = 0x128, ROR4 = 0x124;
__device__ __forceinline__ float x16_sum(float x) {
  auto s = __builtin_amdgcn_permlane16_swap(__float_as_uint(x), __float_as_uint(x), false, false);
  return __uint_as_float(s[0]) + __uint_as_float(s[1]);
}
__device__ __forceinline__ float x32_sum(float x) {
  auto s = __builtin_amdgcn_permlane32_swap(__float_as_uint(x), __float_as_uint(x), false, false);
  return __uint_as_float(s[0]) + __uint_as_float(s[1]);
}
__device__ __forceinline__ float x16_max(float x) {
  auto s = __builtin_amdgcn_permlane16_swap(__float_as_uint(x), __float_as_uint(x), false, false);
  return fmaxf(__uint_as_float(s[0]), __uint_as_float(s[1]));
}
__device__ __forceinline__ float x32_max(float x) {
  auto s = __builtin_amdgcn_permlane32_swap(__float_as_uint(x), __float_as_uint(x), false, false);
  return fmaxf(__uint_as_float(s[0]), __uint_as_float(s[1]));
}
__device__ __forceinline__ float wave_sum(float x) {
  x += dpp<XOR1>(x); x += dpp<XOR2>(x); x += dpp<XOR7>(x); x += dpp<XOR8>(x);
  x = x16_sum(x); x = x32_sum(x); return x;
}

constexpr size_t WS_Q = 0;
constexpr size_t WS_H = 4096;
constexpr size_t WS_ML = 8192;
constexpr size_t WS_O = 16384;
constexpr size_t WS_P = WS_O + 32 * 64 * 128;

__device__ __forceinline__ void ln_stats(const unsigned short* x, int lane, float& mean, float& rstd) {
  const v4u a = *(const v4u*)(x + lane * 16), b = *(const v4u*)(x + lane * 16 + 8);
  float v[16];
#pragma unroll
  for (int i = 0; i < 4; ++i) { v[2 * i] = bf_lo(a[i]); v[2 * i + 1] = bf_hi(a[i]); v[8 + 2 * i] = bf_lo(b[i]); v[9 + 2 * i] = bf_hi(b[i]); }
  float s = 0.f;
#pragma unroll
  for (int i = 0; i < 16; ++i) s += v[i];
  s = wave_sum(s);
  mean = s * (1.0f / D);
  float q = 0.f;
#pragma unroll
  for (int i = 0; i < 16; ++i) { const float d = v[i] - mean; q += d * d; }
  q = wave_sum(q);
  rstd = 1.0f / sqrtf(q * (1.0f / D) + 1e-5f);
}

__device__ __forceinline__ void gemv16_acc(const v4u (&wv)[4], const float (&xn)[4], float (&acc)[8]) {
#pragma unroll
  for (int i = 0; i < 8; ++i) acc[i] = 0.f;
#pragma unroll
  for (int t = 0; t < 4; ++t)
#pragma unroll
    for (int d = 0; d < 4; ++d) { acc[2 * d] = fmaf(xn[t], bf_lo(wv[t][d]), acc[2 * d]); acc[2 * d + 1] = fmaf(xn[t], bf_hi(wv[t][d]), acc[2 * d + 1]); }
}
__device__ __forceinline__ float par_sum(float x) {
  x += dpp<XOR2>(x);
  x += dpp<ROR4>(x);
  x += dpp<XOR8>(x);
  x = x16_sum(x); x = x32_sum(x); return x;
}

__device__ __forceinline__ float gelu_tanh(float x) {
  const float u = 0.7978845608028654f * (x + 0.044715f * x * x * x);
  const float e = __expf(2.f * u);
  const float t = 1.f - 2.f / (e + 1.f);
  return 0.5f * x * (1.f + t);
}

__global__ void __launch_bounds__(NTHR) k1_proj(const unsigned short* x, const unsigned short* wq, const unsigned short* wg, const unsigned short* wu, float* ws) {
  __shared__ float red[3][8][16];
  const int b = blockIdx.x, tid = threadIdx.x, lane = tid & 63, w = tid >> 6;
  v4u a[4], g[4], u[4];
  const bool ffn = b < FF / 16;
#pragma unroll
  for (int t = 0; t < 4; ++t) {
    const int row = 128 * w + 32 * t + (lane >> 1);
    a[t] = ldnt((const v4u*)(wq + (size_t)row * 4096 + 16 * b + 8 * (lane & 1)));
    if (ffn) {
      g[t] = ldnt((const v4u*)(wg + (size_t)row * FF + 16 * b + 8 * (lane & 1)));
      u[t] = ldnt((const v4u*)(wu + (size_t)row * FF + 16 * b + 8 * (lane & 1)));
    }
  }
  float mean, rstd;
  ln_stats(x, lane, mean, rstd);
  float xn[4];
#pragma unroll
  for (int t = 0; t < 4; ++t) xn[t] = (__builtin_bit_cast(float, (unsigned)x[128 * w + 32 * t + (lane >> 1)] << 16) - mean) * rstd;
  float acc[8];
  gemv16_acc(a, xn, acc);
#pragma unroll
  for (int i = 0; i < 8; ++i) { const float s = par_sum(acc[i]); if (lane < 2) red[0][w][8 * lane + i] = s; }
  if (ffn) {
    gemv16_acc(g, xn, acc);
#pragma unroll
    for (int i = 0; i < 8; ++i) { const float s = par_sum(acc[i]); if (lane < 2) red[1][w][8 * lane + i] = s; }
    gemv16_acc(u, xn, acc);
#pragma unroll
    for (int i = 0; i < 8; ++i) { const float s = par_sum(acc[i]); if (lane < 2) red[2][w][8 * lane + i] = s; }
  }
  __syncthreads();
  if (tid < 16) {
    float s = 0.f, sg = 0.f, su = 0.f;
#pragma unroll
    for (int i = 0; i < 8; ++i) { s += red[0][i][tid]; if (ffn) { sg += red[1][i][tid]; su += red[2][i][tid]; } }
    ws[WS_Q + 16 * b + tid] = s;
    if (ffn) ws[WS_H + 16 * b + tid] = gelu_tanh(sg) * su;
  }
}

#define QHILO 1

__global__ void __launch_bounds__(NTHR, 2) k2_attn(const unsigned short* kc, const unsigned short* vc, float* ws) {
  __shared__ __attribute__((aligned(16))) float osm[8][8][128];
  __shared__ float msm[8][8], lsm[8][8];
  const int b = blockIdx.x, tid = threadIdx.x, lane = tid & 63, w = tid >> 6;
  const int kvh = b >> 6, split = b & 63;
  const int c = lane & 15, g4 = lane >> 4;
  const size_t key0 = (size_t)kvh * SK + split * 1024 + w * 128;
  const v4u* kp = (const v4u*)(kc + key0 * DH);
  const v4u* vp = (const v4u*)(vc + key0 * DH);
  v4u K[32], V[32];
  const int krow = 4 * (c & 3) + (c >> 2);
#pragma unroll
  for (int tau = 0; tau < 8; ++tau)
#pragma unroll
    for (int s4 = 0; s4 < 4; ++s4) K[tau * 4 + s4] = ldnt(kp + (16 * tau + krow) * 16 + 4 * s4 + g4);
  __builtin_amdgcn_sched_barrier(0);
  v4u Q[4];
  const float* qsrc = ws + WS_Q + (kvh * 8 + (c & 7)) * DH;
#pragma unroll
  for (int s4 = 0; s4 < 4; ++s4) {
    const f32x4 a = *(const f32x4*)(qsrc + 32 * s4 + 8 * g4), bq = *(const f32x4*)(qsrc + 32 * s4 + 8 * g4 + 4);
    float f[8] = {a[0], a[1], a[2], a[3], bq[0], bq[1], bq[2], bq[3]};
    unsigned hi[4], lo[4];
#pragma unroll
    for (int j = 0; j < 4; ++j) {
      hi[j] = cvtpk(f[2 * j], f[2 * j + 1]);
      lo[j] = QHILO ? cvtpk(f[2 * j] - bf_lo(hi[j]), f[2 * j + 1] - bf_hi(hi[j])) : 0u;
    }
#pragma unroll
    for (int j = 0; j < 4; ++j) Q[s4][j] = (c < 8) ? hi[j] : lo[j];
  }
  f32x4 S[8];
#pragma unroll
  for (int tau = 0; tau < 8; ++tau) {
    f32x4 acc = {0.f, 0.f, 0.f, 0.f};
#pragma unroll
    for (int s4 = 0; s4 < 4; ++s4) acc = mfma(K[tau * 4 + s4], Q[s4], acc);
    S[tau] = acc;
#pragma unroll
    for (int j = 0; j < 4; ++j) V[tau * 4 + j] = ldnt(vp + (tau * 4 + j) * 64 + lane);
    __builtin_amdgcn_sched_barrier(0);
  }
  constexpr float C2 = 0.08838834764831845f * 1.4426950408889634f;
  float m = -INFINITY;
#pragma unroll
  for (int tau = 0; tau < 8; ++tau)
#pragma unroll
    for (int i = 0; i < 4; ++i) { float s = S[tau][i]; s += dpp<XOR8>(s); s *= C2; S[tau][i] = s; m = fmaxf(m, s); }
  m = x16_max(m); m = x32_max(m);
  float l = 0.f;
  v4u P[4];
#pragma unroll
  for (int u = 0; u < 4; ++u) {
#pragma unroll
    for (int jj = 0; jj < 4; ++jj) {
      const int tau = 2 * u + (jj >> 1), i0 = 2 * (jj & 1);
      const float p0 = __builtin_amdgcn_exp2f(S[tau][i0] - m), p1 = __builtin_amdgcn_exp2f(S[tau][i0 + 1] - m);
      l += p0 + p1;
      const unsigned hi = cvtpk(p0, p1);
      const unsigned lo = cvtpk(p0 - bf_lo(hi), p1 - bf_hi(hi));
      P[u][jj] = (c < 8) ? hi : lo;
    }
  }
  l = x16_sum(l); l = x32_sum(l);
  f32x4 O[8];
#pragma unroll
  for (int i = 0; i < 8; ++i) O[i] = f32x4{0.f, 0.f, 0.f, 0.f};
#pragma unroll
  for (int u = 0; u < 4; ++u) {
#pragma unroll
    for (int i = 0; i < 8; ++i) {
      v4u bfrag;
#pragma unroll
      for (int jj = 0; jj < 4; ++jj)
        bfrag[jj] = __builtin_amdgcn_perm(V[8 * u + 2 * jj + 1][i >> 1], V[8 * u + 2 * jj][i >> 1], (i & 1) ? 0x07060302u : 0x05040100u);
      O[i] = mfma(P[u], bfrag, O[i]);
    }
  }
#pragma unroll
  for (int i = 0; i < 8; ++i)
#pragma unroll
    for (int r = 0; r < 4; ++r) O[i][r] = x32_sum(O[i][r]);
  if (g4 < 2) {
#pragma unroll
    for (int r = 0; r < 4; ++r) {
      float* dst = &osm[w][4 * g4 + r][8 * c];
      *(f32x4*)dst = f32x4{O[0][r], O[1][r], O[2][r], O[3][r]};
      *(f32x4*)(dst + 4) = f32x4{O[4][r], O[5][r], O[6][r], O[7][r]};
    }
  }
  if (lane < 8) { msm[w][lane] = m; lsm[w][lane] = l; }
  __syncthreads();
  {
    const int h = w;
    float M = -INFINITY;
#pragma unroll
    for (int i = 0; i < 8; ++i) M = fmaxf(M, msm[i][h]);
    float L = 0.f, o0 = 0.f, o1 = 0.f;
#pragma unroll
    for (int i = 0; i < 8; ++i) {
      const float sc = __builtin_amdgcn_exp2f(msm[i][h] - M);
      L = fmaf(sc, lsm[i][h], L);
      o0 = fmaf(sc, osm[i][h][2 * lane], o0);
      o1 = fmaf(sc, osm[i][h][2 * lane + 1], o1);
    }
    const int head = kvh * 8 + h;
    float* od = ws + WS_O + ((size_t)head * 64 + split) * 128 + 2 * lane;
    od[0] = o0; od[1] = o1;
    if (lane == 0) { ws[WS_ML + (head * 64 + split) * 2] = M; ws[WS_ML + (head * 64 + split) * 2 + 1] = L; }
  }
}

__global__ void __launch_bounds__(NTHR) k3_oproj(const unsigned short* wo, const unsigned short* wd, float* ws) {
  __shared__ float osh[16];
  const int b = blockIdx.x, tid = threadIdx.x, lane = tid & 63, w = tid >> 6;
  const int c = lane & 15, g4 = lane >> 4;
  const bool ffn = b < FF / 16;
  v4u a[4], dn[4];
#pragma unroll
  for (int t = 0; t < 4; ++t) {
    a[t] = ldnt((const v4u*)(wo + (size_t)(16 * b + 4 * t + g4) * D + 128 * w + 8 * c));
    if (ffn) dn[t] = ldnt((const v4u*)(wd + (size_t)(16 * b + 4 * t + g4) * D + 128 * w + 8 * c));
  }
  {
    const int head = b >> 3, d0 = 16 * (b & 7) + 2 * w;
    const float m = ws[WS_ML + (head * 64 + lane) * 2], l = ws[WS_ML + (head * 64 + lane) * 2 + 1];
    const float o0 = ws[WS_O + ((size_t)head * 64 + lane) * 128 + d0], o1 = ws[WS_O + ((size_t)head * 64 + lane) * 128 + d0 + 1];
    float M = m;
    M = fmaxf(M, dpp<XOR1>(M)); M = fmaxf(M, dpp<XOR2>(M)); M = fmaxf(M, dpp<XOR7>(M)); M = fmaxf(M, dpp<XOR8>(M));
    M = x16_max(M); M = x32_max(M);
    const float sc = __builtin_amdgcn_exp2f(m - M);
    const float L = wave_sum(sc * l), a0 = wave_sum(sc * o0), a1 = wave_sum(sc * o1);
    if (lane == 0) { osh[2 * w] = a0 / L; osh[2 * w + 1] = a1 / L; }
  }
  __syncthreads();
  float acc[8];
#pragma unroll
  for (int i = 0; i < 8; ++i) acc[i] = 0.f;
#pragma unroll
  for (int t = 0; t < 4; ++t) {
    const float ov = osh[4 * t + g4];
#pragma unroll
    for (int d = 0; d < 4; ++d) { acc[2 * d] = fmaf(ov, bf_lo(a[t][d]), acc[2 * d]); acc[2 * d + 1] = fmaf(ov, bf_hi(a[t][d]), acc[2 * d + 1]); }
    if (ffn) {
      const float hv = ws[WS_H + 16 * b + 4 * t + g4];
#pragma unroll
      for (int d = 0; d < 4; ++d) { acc[2 * d] = fmaf(hv, bf_lo(dn[t][d]), acc[2 * d]); acc[2 * d + 1] = fmaf(hv, bf_hi(dn[t][d]), acc[2 * d + 1]); }
    }
  }
#pragma unroll
  for (int i = 0; i < 8; ++i) { acc[i] = x16_sum(acc[i]); acc[i] = x32_sum(acc[i]); }
  if (g4 == 0) {
    float* dst = ws + WS_P + (size_t)b * 1024 + 128 * w + 8 * c;
    *(f32x4*)dst = f32x4{acc[0], acc[1], acc[2], acc[3]};
    *(f32x4*)(dst + 4) = f32x4{acc[4], acc[5], acc[6], acc[7]};
  }
}

__global__ void __launch_bounds__(256) k4_final(const unsigned short* x, const float* ws, __hip_bfloat16* out) {
  __shared__ float red[4][4];
  const int b = blockIdx.x, tid = threadIdx.x, lane = tid & 63, w = tid >> 6;
  f32x4 v = *(const f32x4*)(ws + WS_P + (size_t)tid * 1024 + 4 * b);
#pragma unroll
  for (int i = 0; i < 4; ++i) { const float s = wave_sum(v[i]); if (lane == 0) red[w][i] = s; }
  __syncthreads();
  if (tid < 4) {
    const float s = red[0][tid] + red[1][tid] + red[2][tid] + red[3][tid];
    const float xv = __builtin_bit_cast(float, (unsigned)x[4 * b + tid] << 16);
    out[4 * b + tid] = __float2bfloat16(xv + s);
  }
}

extern "C" void kernel_launch(void* const* d_in, const int* in_sizes, int n_in, void* d_out, int out_size, void* d_ws, size_t ws_size,
                              hipStream_t stream) {
  const unsigned short* x = (const unsigned short*)d_in[0];
  const unsigned short* kc = (const unsigned short*)d_in[1];
  const unsigned short* vc = (const unsigned short*)d_in[2];
  const unsigned short* wq = (const unsigned short*)d_in[3];
  const unsigned short* wo = (const unsigned short*)d_in[4];
  const unsigned short* wg = (const unsigned short*)d_in[6];
  const unsigned short* wu = (const unsigned short*)d_in[7];
  const unsigned short* wd = (const unsigned short*)d_in[8];
  float* ws = (float*)d_ws;
  k1_proj<<<NWG, NTHR, 0, stream>>>(x, wq, wg, wu, ws);
  k2_attn<<<NWG, NTHR, 0, stream>>>(kc, vc, ws);
  k3_oproj<<<NWG, NTHR, 0, stream>>>(wo, wd, ws);
  k4_final<<<NWG, 256, 0, stream>>>(x, ws, (__hip_bfloat16*)d_out);
}
```

```cpp
#include <hip/hip_runtime.h>
#include <hip/hip_bf16.h>
#include <stdint.h>

#define STAMPS 0
#define WIN 8

typedef unsigned v4u __attribute__((ext_vector_type(4)));
typedef unsigned v2u __attribute__((ext_vector_type(2)));
typedef float f32x4 __attribute__((ext_vector_type(4)));
typedef float f32x2 __attribute__((ext_vector_type(2)));
typedef short bf16x8 __attribute__((ext_vector_type(8)));
typedef __bf16 bf16x2_t __attribute__((ext_vector_type(2)));
#define LAS __attribute__((address_space(3)))

constexpr int D = 1024, SK = 65536, DH = 128, FF = 3584;
constexpr int NWG = 256, NTHR = 512, NFFN = 224;
constexpr int AUX_SC1 = 16;

constexpr int WS_EPOCH = 0;
constexpr int WS_Q = 4096;
constexpr int WS_F = WS_Q + 65536;
constexpr int WS_A = WS_F + 56 * 4 * 4 * 256;
constexpr int WS_Z = WS_A + 4 * 64 * 8 * 64 * 16;
constexpr int WS_END = WS_Z + 256 * 1024 * 8;
constexpr int WS_STAMP = 8 << 20;

__device__ __forceinline__ float bf_lo(unsigned u) { return __builtin_bit_cast(float, u << 16); }
__device__ __forceinline__ float bf_hi(unsigned u) { return __builtin_bit_cast(float, u & 0xffff0000u); }
__device__ __forceinline__ unsigned cvtpk(float lo, float hi) {
  return __builtin_bit_cast(unsigned, __builtin_convertvector(f32x2{lo, hi}, bf16x2_t));
}
__device__ __forceinline__ unsigned f2u(float f) { return __builtin_bit_cast(unsigned, f); }
__device__ __forceinline__ float u2f(unsigned u) { return __builtin_bit_cast(float, u); }
__device__ __forceinline__ f32x4 mfma(v4u a, v4u b, f32x4 c) {
  return __builtin_amdgcn_mfma_f32_16x16x32_bf16(__builtin_bit_cast(bf16x8, a), __builtin_bit_cast(bf16x8, b), c, 0, 0, 0);
}
template <int CTRL> __device__ __forceinline__ float dpp(float x) {
  return __builtin_bit_cast(float, __builtin_amdgcn_mov_dpp(__builtin_bit_cast(int, x), CTRL, 0xf, 0xf, true));
}
constexpr int XOR1 = 0xB1, XOR2 = 0x4E, XOR7 = 0x141, XOR8 = 0x128, ROR4 = 0x124;
__device__ __forceinline__ float x16_sum(float x) {
  auto s = __builtin_amdgcn_permlane16_swap(f2u(x), f2u(x), false, false);
  return u2f(s[0]) + u2f(s[1]);
}
__device__ __forceinline__ float x32_sum(float x) {
  auto s = __builtin_amdgcn_permlane32_swap(f2u(x), f2u(x), false, false);
  return u2f(s[0]) + u2f(s[1]);
}
__device__ __forceinline__ float x16_max(float x) {
  auto s = __builtin_amdgcn_permlane16_swap(f2u(x), f2u(x), false, false);
  return fmaxf(u2f(s[0]), u2f(s[1]));
}
__device__ __forceinline__ float x32_max(float x) {
  auto s = __builtin_amdgcn_permlane32_swap(f2u(x), f2u(x), false, false);
  return fmaxf(u2f(s[0]), u2f(s[1]));
}
__device__ __forceinline__ float wave_sum(float x) {
  x += dpp<XOR1>(x); x += dpp<XOR2>(x); x += dpp<XOR7>(x); x += dpp<XOR8>(x);
  x = x16_sum(x); x = x32_sum(x); return x;
}
__device__ __forceinline__ float hi3_sum(float x) { x += dpp<XOR8>(x); x = x16_sum(x); x = x32_sum(x); return x; }
__device__ __forceinline__ float hi3_max(float x) { x = fmaxf(x, dpp<XOR8>(x)); x = x16_max(x); x = x32_max(x); return x; }

__device__ __forceinline__ float gelu_tanh(float x) {
  const float u = 0.7978845608028654f * (x + 0.044715f * x * x * x);
  const float e = __expf(2.f * u);
  const float t = 1.f - 2.f / (e + 1.f);
  return 0.5f * x * (1.f + t);
}

#define SB() __builtin_amdgcn_sched_barrier(0)
#define STR2(x) #x
#define STR(x) STR2(x)
#define PACE(n) do { SB(); asm volatile("s_waitcnt vmcnt(" STR(n) ")" ::: "memory"); SB(); } while (0)
#define BARRIER() do { asm volatile("s_waitcnt lgkmcnt(0)" ::: "memory"); __builtin_amdgcn_s_barrier(); asm volatile("" ::: "memory"); } while (0)
#define MKRS(p, n) __builtin_amdgcn_make_buffer_rsrc((void*)(p), 0, (n), 0x00020000)
#define LDNT(r, voff, soff) __builtin_amdgcn_raw_buffer_load_b128((r), (voff), (soff), 2)
#define LD_SC1(off) __builtin_amdgcn_raw_buffer_load_b128(rs, (off), 0, AUX_SC1)
#define ST_SC1(v, off) __builtin_amdgcn_raw_buffer_store_b128((v), rs, (off), 0, AUX_SC1)
constexpr unsigned SPIN_CAP = 1u << 18;
#define POLL_WAIT(cond, reload) do { if (__builtin_expect(!__all(cond), 0)) { unsigned spins_ = 0; do { __builtin_amdgcn_s_sleep(1); asm volatile("" ::: "memory"); reload; } while (!__all(cond) && ++spins_ < SPIN_CAP); } } while (0)
#define ARRIVE(k) do { asm volatile("s_waitcnt lgkmcnt(0)" ::: "memory"); if (lane == 0) __hip_atomic_fetch_add(&L.cnt[k], 1u, __ATOMIC_RELAXED, __HIP_MEMORY_SCOPE_WORKGROUP); asm volatile("" ::: "memory"); } while (0)
#define WAITN(k, n) do { asm volatile("" ::: "memory"); while (__hip_atomic_load(&L.cnt[k], __ATOMIC_RELAXED, __HIP_MEMORY_SCOPE_WORKGROUP) < (n)) { __builtin_amdgcn_s_sleep(0); } asm volatile("" ::: "memory"); } while (0)

#define PACE2() PACE(6)

#define STAMP(i) do { } while (0)

__device__ __forceinline__ void fma8(float (&acc)[8], float xs, v4u wv) {
#pragma unroll
  for (int d = 0; d < 4; ++d) { acc[2 * d] = fmaf(xs, bf_lo(wv[d]), acc[2 * d]); acc[2 * d + 1] = fmaf(xs, bf_hi(wv[d]), acc[2 * d + 1]); }
}

struct Lds {
  float red[8][128];
  float redq[8][64];
  float qs[2][1024];
  float osm[8][8][132];
  float msm[8][8], lsm[8][8];
  float cmb[8][20];
  float vec[32];
  float zred[8][4];
  unsigned cnt[8];
};

__global__ void __launch_bounds__(NTHR, 2)
fused_decode_kernel(const unsigned short* __restrict__ x, const unsigned short* __restrict__ kc, const unsigned short* __restrict__ vc,
                    const unsigned short* __restrict__ wq, const unsigned short* __restrict__ wo, const unsigned short* __restrict__ wg,
                    const unsigned short* __restrict__ wu, const unsigned short* __restrict__ wd, unsigned char* ws, unsigned short* __restrict__ out) {
  __shared__ __attribute__((aligned(16))) Lds L;
  const int b = blockIdx.x, tid = threadIdx.x, lane = tid & 63;
  const int w = __builtin_amdgcn_readfirstlane(tid >> 6);
  const int grp = b >> 6, split = b & 63, kb = b & 3;
  const int c = lane & 15, g4 = lane >> 4, l8 = lane & 7, r8 = lane >> 3;
  const bool ffn = b < NFFN;
  const __amdgpu_buffer_rsrc_t rs = MKRS(ws, WS_END);
  STAMP(0);
  const int row0 = 256 * kb + 32 * w;
  const __amdgpu_buffer_rsrc_t rx = MKRS(x, 2048);
  const v4u xa = __builtin_amdgcn_raw_buffer_load_b128(rx, lane * 32, 0, 0), xb = __builtin_amdgcn_raw_buffer_load_b128(rx, lane * 32 + 16, 0, 0);
  unsigned short xr[4];
#pragma unroll
  for (int t = 0; t < 4; ++t) xr[t] = __builtin_amdgcn_raw_buffer_load_b16(rx, r8 * 2 + 16 * t, row0 * 2, 0);
  const unsigned xres = __builtin_amdgcn_raw_buffer_load_b32(rx, (lane & 1) * 4, b * 8, 0);
  const unsigned tag = __builtin_amdgcn_raw_buffer_load_b32(rs, WS_EPOCH, 0, AUX_SC1) + 1u;
  const __amdgpu_buffer_rsrc_t rwq = MKRS(wq + (size_t)row0 * 4096 + 1024 * grp + 64 * (split >> 2), 32 * 8192);
  v4u wqv[4];
#pragma unroll
  for (int t = 0; t < 4; ++t) wqv[t] = LDNT(rwq, r8 * 8192 + l8 * 16, t * 65536);
  if (tid < 8) L.cnt[tid] = 0u;
  SB();
  BARRIER();
  const size_t key0 = (size_t)grp * SK + (split * 8 + w) * 32;
  const __amdgpu_buffer_rsrc_t rk = MKRS(kc + key0 * DH, 3 * (4 << 20) + 8192), rv = MKRS(vc + key0 * DH, 3 * (4 << 20) + 8192);
  const int kvo = (4 * (c & 3) + ((c >> 2) & 1)) * 256 + (c >> 3) * 64 + g4 * 16, vvo = lane * 16;
#define LDK(i) LDNT(rk, kvo + (((i) >> 1) & 1) * 512 + ((i) & 1) * 128, ((i) >> 3) * (4 << 20) + (((i) >> 2) & 1) * 4096)
#define LDV(i) LDNT(rv, vvo + ((i) & 3) * 1024, ((i) >> 3) * (4 << 20) + (((i) >> 2) & 1) * 4096)
  v4u K[32], V[32];
#pragma unroll
  for (int i = 0; i < 8; i += 2) { K[i] = LDK(i); K[i + 1] = LDK(i + 1); SB(); }
  float xn[4];
  {
    float v[16];
#pragma unroll
    for (int i = 0; i < 4; ++i) { v[2 * i] = bf_lo(xa[i]); v[2 * i + 1] = bf_hi(xa[i]); v[8 + 2 * i] = bf_lo(xb[i]); v[9 + 2 * i] = bf_hi(xb[i]); }
    float s = 0.f;
#pragma unroll
    for (int i = 0; i < 16; ++i) s += v[i];
    const float mean = wave_sum(s) * (1.0f / D);
    float q2 = 0.f;
#pragma unroll
    for (int i = 0; i < 16; ++i) { const float d = v[i] - mean; q2 = fmaf(d, d, q2); }
    const float rstd = 1.0f / sqrtf(wave_sum(q2) * (1.0f / D) + 1e-5f);
#pragma unroll
    for (int t = 0; t < 4; ++t) xn[t] = (bf_lo(xr[t]) - mean) * rstd;
  }
  {
    float acc[8];
#pragma unroll
    for (int i = 0; i < 8; ++i) acc[i] = 0.f;
#pragma unroll
    for (int t = 0; t < 4; ++t) fma8(acc, xn[t], wqv[t]);
#pragma unroll
    for (int i = 0; i < 8; ++i) acc[i] = hi3_sum(acc[i]);
    if (lane < 8) {
      *(f32x4*)&L.redq[w][8 * lane] = f32x4{acc[0], acc[1], acc[2], acc[3]};
      *(f32x4*)&L.redq[w][8 * lane + 4] = f32x4{acc[4], acc[5], acc[6], acc[7]};
    }
  }
  ARRIVE(0);
  STAMP(1);
  if (w == 0) {
    WAITN(0, 8u);
    if (lane < 16) {
      f32x4 s = {0.f, 0.f, 0.f, 0.f};
#pragma unroll
      for (int i = 0; i < 8; ++i) s += *(const f32x4*)&L.redq[i][4 * lane];
      const v4u g = {cvtpk(s[0], s[1]), tag, cvtpk(s[2], s[3]), tag};
      ST_SC1(g, WS_Q + ((grp * 4 + kb) * 512 + (split >> 2) * 32) * 8 + lane * 16);
    }
  }
  SB();
#pragma unroll
  for (int i = 8; i < 20; i += 2) { PACE2(); K[i] = LDK(i); K[i + 1] = LDK(i + 1); }
  const int qoff = WS_Q + (grp * 4 + 2 * (tid >> 8)) * 4096 + (tid & 255) * 16;
  PACE2();
  v4u qa = LD_SC1(qoff), qb = LD_SC1(qoff + 4096);
#pragma unroll
  for (int i = 20; i < 30; i += 2) { PACE2(); K[i] = LDK(i); K[i + 1] = LDK(i + 1); }
  SB();
  if (__builtin_expect(!__all(qa[1] == tag && qa[3] == tag && qb[1] == tag && qb[3] == tag), 0)) { qa = LD_SC1(qoff); qb = LD_SC1(qoff + 4096); }
  SB();
#pragma unroll
  for (int i = 30; i < 32; i += 2) { PACE2(); K[i] = LDK(i); K[i + 1] = LDK(i + 1); }
#pragma unroll
  for (int i = 0; i < 6; i += 2) { PACE2(); V[i] = LDV(i); V[i + 1] = LDV(i + 1); }
  SB();
  POLL_WAIT(qa[1] == tag && qa[3] == tag && qb[1] == tag && qb[3] == tag, { qa = LD_SC1(qoff); qb = LD_SC1(qoff + 4096); });
  {
    float* qs = &L.qs[0][0];
    const f32x4 s = {bf_lo(qa[0]) + bf_lo(qb[0]), bf_hi(qa[0]) + bf_hi(qb[0]), bf_lo(qa[2]) + bf_lo(qb[2]), bf_hi(qa[2]) + bf_hi(qb[2])};
    *(f32x4*)&qs[(tid >> 8) * 1024 + (tid & 255) * 4] = s;
  }
  ARRIVE(1);
  STAMP(2);
#pragma unroll
  for (int i = 6; i < 10; i += 2) { PACE2(); V[i] = LDV(i); V[i + 1] = LDV(i + 1); }
  SB();
  WAITN(1, 8u);
  v4u Q[2];
  {
    const float* qs = &L.qs[0][0] + (c & 7) * DH + 32 * (c >> 3) + 8 * g4;
#pragma unroll
    for (int ln = 0; ln < 2; ++ln) {
      const f32x4 f0 = *(const f32x4*)(qs + 64 * ln) + *(const f32x4*)(qs + 1024 + 64 * ln);
      const f32x4 f1 = *(const f32x4*)(qs + 64 * ln + 4) + *(const f32x4*)(qs + 1024 + 64 * ln + 4);
      Q[ln] = v4u{cvtpk(f0[0], f0[1]), cvtpk(f0[2], f0[3]), cvtpk(f1[0], f1[1]), cvtpk(f1[2], f1[3])};
    }
  }
  constexpr float C2 = 0.08838834764831845f * 1.4426950408889634f;
  float S[8][4];
  float m = -INFINITY;
#pragma unroll
  for (int p = 0; p < 8; ++p) {
    f32x4 X = {0.f, 0.f, 0.f, 0.f}, Y = {0.f, 0.f, 0.f, 0.f};
    X = mfma(K[4 * p], Q[0], X); Y = mfma(K[4 * p + 2], Q[0], Y);
    X = mfma(K[4 * p + 1], Q[1], X); Y = mfma(K[4 * p + 3], Q[1], Y);
#pragma unroll
    for (int i = 0; i < 4; ++i) {
      auto sw = __builtin_amdgcn_permlane32_swap(f2u(X[i]), f2u(Y[i]), false, false);
      const float s = u2f(sw[0]) + dpp<XOR8>(u2f(sw[1]));
      S[p][i] = s; m = fmaxf(m, s);
    }
  }
  SB();
  V[10] = LDV(10); V[11] = LDV(11);
  SB();
  STAMP(3);
  m = x16_max(m); m = x32_max(m);
  const float mc = m * C2;
  float l = 0.f;
  v4u P[4];
#pragma unroll
  for (int u = 0; u < 4; ++u)
#pragma unroll
    for (int jj = 0; jj < 4; ++jj) {
      const int p = 2 * u + (jj >> 1), i0 = 2 * (jj & 1);
      const float p0 = __builtin_amdgcn_exp2f(fmaf(S[p][i0], C2, -mc)), p1 = __builtin_amdgcn_exp2f(fmaf(S[p][i0 + 1], C2, -mc));
      const unsigned pk = cvtpk(p0, p1);
      l += bf_lo(pk) + bf_hi(pk);
      P[u][jj] = pk;
    }
  l = x16_sum(l); l = x32_sum(l);
  if (lane < 8) { L.msm[lane][w] = mc; L.lsm[lane][w] = l; }
  f32x4 O[8];
#pragma unroll
  for (int i = 0; i < 8; ++i) O[i] = f32x4{0.f, 0.f, 0.f, 0.f};
#define PV(u) do { _Pragma("unroll") for (int i = 0; i < 8; ++i) { v4u bfrag; \
      _Pragma("unroll") for (int jj = 0; jj < 4; ++jj) \
        bfrag[jj] = __builtin_amdgcn_perm(V[8 * (u) + 2 * jj + 1][i >> 1], V[8 * (u) + 2 * jj][i >> 1], (i & 1) ? 0x07060302u : 0x05040100u); \
      O[i] = mfma(P[u], bfrag, O[i]); } SB(); } while (0)
#pragma unroll
  for (int i = 12; i < 16; i += 2) { PACE2(); V[i] = LDV(i); V[i + 1] = LDV(i + 1); }
  SB();
  PV(0);
#pragma unroll
  for (int i = 16; i < 24; i += 2) { PACE2(); V[i] = LDV(i); V[i + 1] = LDV(i + 1); }
  SB();
  PV(1);
#pragma unroll
  for (int i = 24; i < 32; i += 2) { PACE2(); V[i] = LDV(i); V[i + 1] = LDV(i + 1); }
  SB();
  PV(2);
  v4u gv[4], uv[4], wov[4], wdv[4];
  {
    const int fb = ffn ? (b >> 2) : 0, fsz = ffn ? 32 * FF * 2 - 128 * fb : 0;
    const __amdgpu_buffer_rsrc_t rg = MKRS(wg + (size_t)row0 * FF + 64 * fb, fsz), ru = MKRS(wu + (size_t)row0 * FF + 64 * fb, fsz);
#pragma unroll
    for (int t = 0; t < 4; ++t) {
      PACE2();
      gv[t] = LDNT(rg, r8 * (FF * 2) + l8 * 16, t * (8 * FF * 2));
      uv[t] = LDNT(ru, r8 * (FF * 2) + l8 * 16, t * (8 * FF * 2));
    }
  }
  SB();
  PV(3);
  STAMP(4);
  if (g4 < 2) {
#pragma unroll
    for (int r = 0; r < 4; ++r) {
      float* dst = &L.osm[w][4 * g4 + r][8 * c];
      *(f32x4*)dst = f32x4{O[0][r], O[1][r], O[2][r], O[3][r]};
      *(f32x4*)(dst + 4) = f32x4{O[4][r], O[5][r], O[6][r], O[7][r]};
    }
  }
  ARRIVE(2);
  const __amdgpu_buffer_rsrc_t rwo = MKRS(wo + (size_t)(16 * b) * D + 128 * w, 32768 - 256 * w);
  const __amdgpu_buffer_rsrc_t rwd = MKRS(wd + (size_t)(ffn ? 16 * b : 0) * D + 128 * w, ffn ? 32768 - 256 * w : 0);
  wov[0] = LDNT(rwo, g4 * 2048 + c * 16, 0 * 8192); wov[1] = LDNT(rwo, g4 * 2048 + c * 16, 1 * 8192);
  SB();
  WAITN(2, 8u);
  {
    const f32x4 m0 = *(const f32x4*)&L.msm[w][0], m1 = *(const f32x4*)&L.msm[w][4];
    const f32x4 l0 = *(const f32x4*)&L.lsm[w][0], l1 = *(const f32x4*)&L.lsm[w][4];
    const float mm[8] = {m0[0], m0[1], m0[2], m0[3], m1[0], m1[1], m1[2], m1[3]};
    const float ll[8] = {l0[0], l0[1], l0[2], l0[3], l1[0], l1[1], l1[2], l1[3]};
    float M = mm[0];
#pragma unroll
    for (int i = 1; i < 8; ++i) M = fmaxf(M, mm[i]);
    float Ls = 0.f, o0 = 0.f, o1 = 0.f;
#pragma unroll
    for (int i = 0; i < 8; ++i) {
      const float sc = __builtin_amdgcn_exp2f(mm[i] - M);
      const f32x2 ov = *(const f32x2*)&L.osm[i][w][2 * lane];
      Ls = fmaf(sc, ll[i], Ls);
      o0 = fmaf(sc, ov[0], o0);
      o1 = fmaf(sc, ov[1], o1);
    }
    const float inv = __builtin_amdgcn_rcpf(Ls);
    const float lse = M + __builtin_amdgcn_logf(Ls);
    const v4u g = {cvtpk(o0 * inv, o1 * inv), tag, f2u(lse), tag};
    ST_SC1(g, WS_A + (((grp * 64 + split) * 8 + w) * 64 + lane) * 16);
  }
  SB();
  STAMP(5);
  PACE2();
  wov[2] = LDNT(rwo, g4 * 2048 + c * 16, 2 * 8192); wov[3] = LDNT(rwo, g4 * 2048 + c * 16, 3 * 8192);
  const int aoff = WS_A + (((grp * 64 + 8 * w + r8) * 8 + (split >> 3)) * 64 + (split & 7) * 8 + l8) * 16;
  PACE2();
  wdv[0] = LDNT(rwd, g4 * 2048 + c * 16, 0 * 8192); wdv[1] = LDNT(rwd, g4 * 2048 + c * 16, 1 * 8192);
  PACE2();
  v4u av = LD_SC1(aoff);
  wdv[2] = LDNT(rwd, g4 * 2048 + c * 16, 2 * 8192); wdv[3] = LDNT(rwd, g4 * 2048 + c * 16, 3 * 8192);
  SB();
  if (ffn) {
    float ag[8], au[8];
#pragma unroll
    for (int i = 0; i < 8; ++i) { ag[i] = 0.f; au[i] = 0.f; }
#pragma unroll
    for (int t = 0; t < 4; ++t) { fma8(ag, xn[t], gv[t]); fma8(au, xn[t], uv[t]); }
#pragma unroll
    for (int i = 0; i < 8; ++i) { ag[i] = hi3_sum(ag[i]); au[i] = hi3_sum(au[i]); }
    if (lane < 8) {
      *(f32x4*)&L.red[w][8 * lane] = f32x4{ag[0], ag[1], ag[2], ag[3]};
      *(f32x4*)&L.red[w][8 * lane + 4] = f32x4{ag[4], ag[5], ag[6], ag[7]};
      *(f32x4*)&L.red[w][64 + 8 * lane] = f32x4{au[0], au[1], au[2], au[3]};
      *(f32x4*)&L.red[w][64 + 8 * lane + 4] = f32x4{au[4], au[5], au[6], au[7]};
    }
    ARRIVE(3);
    if (w == 0) {
      WAITN(3, 8u);
      f32x2 s = {0.f, 0.f};
#pragma unroll
      for (int i = 0; i < 8; ++i) s += *(const f32x2*)&L.red[i][2 * lane];
      const int col = (2 * lane) & 63, isu = lane >> 5;
      const v4u g = {f2u(s[0]), tag, f2u(s[1]), tag};
      ST_SC1(g, WS_F + (((b >> 2) * 4 + (col >> 4)) * 4 + kb) * 256 + (isu * 16 + (col & 15)) * 8);
    }
  }
  SB();
  STAMP(6);
  const int foff = WS_F + (b * 4) * 256 + lane * 16;
  v4u fv = {0u, tag, 0u, tag};
  if (ffn && w == 1) fv = LD_SC1(foff);
  SB();
  POLL_WAIT(av[1] == tag && av[3] == tag, { av = LD_SC1(aoff); });
  STAMP(7);
  {
    const float lse = u2f(av[2]);
    const float M = hi3_max(lse);
    const float sc = __builtin_amdgcn_exp2f(lse - M);
    const float Ls = hi3_sum(sc), o0 = hi3_sum(sc * bf_lo(av[0])), o1 = hi3_sum(sc * bf_hi(av[0]));
    if (lane < 8) { *(f32x2*)&L.cmb[w][2 * lane] = f32x2{o0, o1}; if (lane == 0) *(f32x2*)&L.cmb[w][16] = f32x2{M, Ls}; }
  }
  ARRIVE(4);
  if (ffn && w == 1) {
    POLL_WAIT(fv[1] == tag && fv[3] == tag, { fv = LD_SC1(foff); });
    float s0 = u2f(fv[0]), s1 = u2f(fv[2]);
    s0 = x16_sum(s0); s0 = x32_sum(s0); s1 = x16_sum(s1); s1 = x32_sum(s1);
    const float u0 = dpp<XOR8>(s0), u1 = dpp<XOR8>(s1);
    if (lane < 8) *(f32x2*)&L.vec[16 + 2 * lane] = f32x2{gelu_tanh(s0) * u0, gelu_tanh(s1) * u1};
    ARRIVE(5);
  }
  WAITN(4, 8u);
  float acc[8];
#pragma unroll
  for (int i = 0; i < 8; ++i) acc[i] = 0.f;
  {
    float M = -INFINITY;
#pragma unroll
    for (int i = 0; i < 8; ++i) M = fmaxf(M, L.cmb[i][16]);
    float Ls = 0.f, ov[4] = {0.f, 0.f, 0.f, 0.f};
#pragma unroll
    for (int i = 0; i < 8; ++i) {
      const float sc = __builtin_amdgcn_exp2f(L.cmb[i][16] - M);
      Ls = fmaf(sc, L.cmb[i][17], Ls);
#pragma unroll
      for (int t = 0; t < 4; ++t) ov[t] = fmaf(sc, L.cmb[i][4 * t + g4], ov[t]);
    }
    const float inv = __builtin_amdgcn_rcpf(Ls);
#pragma unroll
    for (int t = 0; t < 4; ++t) fma8(acc, ov[t] * inv, wov[t]);
  }
  if (ffn) {
    WAITN(5, 1u);
#pragma unroll
    for (int t = 0; t < 4; ++t) fma8(acc, L.vec[16 + 4 * t + g4], wdv[t]);
  }
#pragma unroll
  for (int i = 0; i < 8; ++i) { acc[i] = x16_sum(acc[i]); acc[i] = x32_sum(acc[i]); }
  {
    const float z0 = g4 == 0 ? acc[0] : g4 == 1 ? acc[2] : g4 == 2 ? acc[4] : acc[6];
    const float z1 = g4 == 0 ? acc[1] : g4 == 1 ? acc[3] : g4 == 2 ? acc[5] : acc[7];
    const v4u g = {f2u(z0), tag, f2u(z1), tag};
    ST_SC1(g, WS_Z + (b * 1024 + 128 * w + 8 * c + 2 * g4) * 8);
  }
  SB();
  STAMP(8);
  const int zoff = WS_Z + ((tid >> 1) * 1024 + 4 * b + 2 * (tid & 1)) * 8;
  v4u zv = LD_SC1(zoff);
  POLL_WAIT(zv[1] == tag && zv[3] == tag, { zv = LD_SC1(zoff); });
  STAMP(9);
  {
    float z0 = u2f(zv[0]), z1 = u2f(zv[2]);
    z0 += dpp<XOR2>(z0); z0 += dpp<ROR4>(z0); z0 = hi3_sum(z0);
    z1 += dpp<XOR2>(z1); z1 += dpp<ROR4>(z1); z1 = hi3_sum(z1);
    if (lane < 2) *(f32x2*)&L.zred[w][2 * lane] = f32x2{z0, z1};
  }
  BARRIER();
  if (tid < 2) {
    f32x2 s = {0.f, 0.f};
#pragma unroll
    for (int i = 0; i < 8; ++i) s += *(const f32x2*)&L.zred[i][2 * tid];
    *(unsigned*)(out + 4 * b + 2 * tid) = cvtpk(bf_lo(xres) + s[0], bf_hi(xres) + s[1]);
    if (b == 0 && tid == 0) __builtin_amdgcn_raw_buffer_store_b32(tag, rs, WS_EPOCH, 0, AUX_SC1);
  }
  STAMP(10);
}

extern "C" void kernel_launch(void* const* d_in, const int* in_sizes, int n_in, void* d_out, int out_size, void* d_ws, size_t ws_size,
                              hipStream_t stream) {
  fused_decode_kernel<<<NWG, NTHR, 0, stream>>>((const unsigned short*)d_in[0], (const unsigned short*)d_in[1], (const unsigned short*)d_in[2],
                                                (const unsigned short*)d_in[3], (const unsigned short*)d_in[4], (const unsigned short*)d_in[6],
                                                (const unsigned short*)d_in[7], (const unsigned short*)d_in[8], (unsigned char*)d_ws,
                                                (unsigned short*)d_out);
}
```

```cpp
#include <hip/hip_runtime.h>
#include <hip/hip_bf16.h>
#include <stdint.h>

#define STAMPS 0
#define WIN 6

typedef unsigned v4u __attribute__((ext_vector_type(4)));
typedef unsigned v2u __attribute__((ext_vector_type(2)));
typedef float f32x4 __attribute__((ext_vector_type(4)));
typedef float f32x2 __attribute__((ext_vector_type(2)));
typedef short bf16x8 __attribute__((ext_vector_type(8)));
typedef __bf16 bf16x2_t __attribute__((ext_vector_type(2)));
#define LAS __attribute__((address_space(3)))

constexpr int D = 1024, SK = 65536, DH = 128, FF = 3584;
constexpr int NWG = 256, NTHR = 512, NFFN = 224;
constexpr int AUX_SC1 = 16;

constexpr int WS_EPOCH = 0;
constexpr int WS_Q = 4096;
constexpr int WS_F = WS_Q + 131072;
constexpr int WS_ML = WS_F + 56 * 8 * 1024;
constexpr int A_STR = 2048 + 128, Z_STR = 2048 + 128;
constexpr int WS_A = WS_ML + 256 * 128;
constexpr int WS_Z = WS_A + 256 * A_STR;
constexpr int WS_END = WS_Z + 256 * Z_STR;
constexpr int WS_STAMP = 8 << 20;

__device__ __forceinline__ float bf_lo(unsigned u) { return __builtin_bit_cast(float, u << 16); }
__device__ __forceinline__ float bf_hi(unsigned u) { return __builtin_bit_cast(float, u & 0xffff0000u); }
__device__ __forceinline__ unsigned cvtpk(float lo, float hi) {
  return __builtin_bit_cast(unsigned, __builtin_convertvector(f32x2{lo, hi}, bf16x2_t));
}
__device__ __forceinline__ unsigned f2u(float f) { return __builtin_bit_cast(unsigned, f); }
__device__ __forceinline__ float u2f(unsigned u) { return __builtin_bit_cast(float, u); }
__device__ __forceinline__ f32x4 mfma(v4u a, v4u b, f32x4 c) {
  return __builtin_amdgcn_mfma_f32_16x16x32_bf16(__builtin_bit_cast(bf16x8, a), __builtin_bit_cast(bf16x8, b), c, 0, 0, 0);
}
template <int CTRL> __device__ __forceinline__ float dpp(float x) {
  return __builtin_bit_cast(float, __builtin_amdgcn_mov_dpp(__builtin_bit_cast(int, x), CTRL, 0xf, 0xf, true));
}
constexpr int XOR1 = 0xB1, XOR2 = 0x4E, XOR7 = 0x141, XOR8 = 0x128, ROR4 = 0x124;
__device__ __forceinline__ float x16_sum(float x) {
  auto s = __builtin_amdgcn_permlane16_swap(f2u(x), f2u(x), false, false);
  return u2f(s[0]) + u2f(s[1]);
}
__device__ __forceinline__ float x32_sum(float x) {
  auto s = __builtin_amdgcn_permlane32_swap(f2u(x), f2u(x), false, false);
  return u2f(s[0]) + u2f(s[1]);
}
__device__ __forceinline__ float x16_max(float x) {
  auto s = __builtin_amdgcn_permlane16_swap(f2u(x), f2u(x), false, false);
  return fmaxf(u2f(s[0]), u2f(s[1]));
}
__device__ __forceinline__ float x32_max(float x) {
  auto s = __builtin_amdgcn_permlane32_swap(f2u(x), f2u(x), false, false);
  return fmaxf(u2f(s[0]), u2f(s[1]));
}
__device__ __forceinline__ float wave_sum(float x) {
  x += dpp<XOR1>(x); x += dpp<XOR2>(x); x += dpp<XOR7>(x); x += dpp<XOR8>(x);
  x = x16_sum(x); x = x32_sum(x); return x;
}
__device__ __forceinline__ float hi3_sum(float x) { x += dpp<XOR8>(x); x = x16_sum(x); x = x32_sum(x); return x; }
__device__ __forceinline__ float hi3_max(float x) { x = fmaxf(x, dpp<XOR8>(x)); x = x16_max(x); x = x32_max(x); return x; }

__device__ __forceinline__ float tr32(float a, float b) {
  auto s = __builtin_amdgcn_permlane32_swap(f2u(a), f2u(b), false, false);
  return u2f(s[0]) + u2f(s[1]);
}
__device__ __forceinline__ float tr16(float a, float b) {
  auto s = __builtin_amdgcn_permlane16_swap(f2u(a), f2u(b), false, false);
  return u2f(s[0]) + u2f(s[1]);
}
__device__ __forceinline__ float tr8(float a, float b, bool bit3) {
  const float keep = bit3 ? b : a, send = bit3 ? a : b;
  return keep + dpp<XOR8>(send);
}
__device__ __forceinline__ float tr_red8(const float (&a)[8], bool bit3) {
  const float b0 = tr32(a[0], a[4]), b1 = tr32(a[1], a[5]), b2 = tr32(a[2], a[6]), b3 = tr32(a[3], a[7]);
  const float c0 = tr16(b0, b2), c1 = tr16(b1, b3);
  return tr8(c0, c1, bit3);
}
__device__ __forceinline__ f32x2 tr_red4(const float (&a)[8]) {
  const float b0 = tr32(a[0], a[4]), b1 = tr32(a[1], a[5]), b2 = tr32(a[2], a[6]), b3 = tr32(a[3], a[7]);
  return f32x2{tr16(b0, b2), tr16(b1, b3)};
}
__device__ __forceinline__ float lo3_sum(float x) { x += dpp<XOR1>(x); x += dpp<XOR2>(x); x += dpp<XOR7>(x); return x; }
__device__ __forceinline__ float lo3_max(float x) { x = fmaxf(x, dpp<XOR1>(x)); x = fmaxf(x, dpp<XOR2>(x)); x = fmaxf(x, dpp<XOR7>(x)); return x; }

__device__ __forceinline__ float gelu_tanh(float x) {
  const float u = 0.7978845608028654f * (x + 0.044715f * x * x * x);
  const float e = __expf(2.f * u);
  const float t = 1.f - 2.f / (e + 1.f);
  return 0.5f * x * (1.f + t);
}

#define SB() __builtin_amdgcn_sched_barrier(0)
#define STR2(x) #x
#define STR(x) STR2(x)
#define PACE(n) do { SB(); asm volatile("s_waitcnt vmcnt(" STR(n) ")" ::: "memory"); SB(); } while (0)
#define BARRIER() do { asm volatile("s_waitcnt lgkmcnt(0)" ::: "memory"); __builtin_amdgcn_s_barrier(); asm volatile("" ::: "memory"); } while (0)
#define WQP 0
#define WFP 1
#define WFG 2
#define PRIO2 0
#define DEFER 0
#define TGATE 0
#define LATEWO 0
#define WMP 3
#define ADELAY 30
#define ZDELAY 30
#define TRUNC 0
#define FASTSTART 0
#define SYNCV 3
#define SYNC(bit) do { if (SYNCV & (bit)) { SB(); __builtin_amdgcn_s_barrier(); SB(); } } while (0)
#define MKRS(p, n) __builtin_amdgcn_make_buffer_rsrc((void*)(p), 0, (n), 0x00020000)
#define LDNT(r, voff, soff) __builtin_amdgcn_raw_buffer_load_b128((r), (voff), (soff), 2)
#define LD_SC1(off) __builtin_amdgcn_raw_buffer_load_b128(rs, (off), 0, AUX_SC1)
#define ST_SC1(v, off) __builtin_amdgcn_raw_buffer_store_b128((v), rs, (off), 0, AUX_SC1)
constexpr unsigned SPIN_CAP = 1u << 20;
#define POLL_WAIT(cond, reload) do { if (__builtin_expect(!__all(cond), 0)) { unsigned spins_ = 0; do { asm volatile("" ::: "memory"); if (!(cond)) { reload; } } while (!__all(cond) && ++spins_ < SPIN_CAP); } } while (0)
#define ARRIVE(k) do { asm volatile("s_waitcnt lgkmcnt(0)" ::: "memory"); if (lane == 0) __hip_atomic_fetch_add(&L.cnt[k], 1u, __ATOMIC_RELAXED, __HIP_MEMORY_SCOPE_WORKGROUP); asm volatile("" ::: "memory"); } while (0)
#define WAITN(k, n) do { asm volatile("" ::: "memory"); while (__hip_atomic_load(&L.cnt[k], __ATOMIC_RELAXED, __HIP_MEMORY_SCOPE_WORKGROUP) < (n)) { } asm volatile("" ::: "memory"); } while (0)

#define TAU16 460
#define TOFF 100
#define PACEW() PACE(4)
#define PACE2(n) do { PACEW(); while ((int)((unsigned)__builtin_amdgcn_s_memrealtime() - t0lo) < TOFF + (((n) - WIN) * TAU16) / 16) { } SB(); } while (0)

#define STAMP(i) do { } while (0)

__device__ __forceinline__ void fma8(float (&acc)[8], float xs, v4u wv) {
#pragma unroll
  for (int d = 0; d < 4; ++d) { acc[2 * d] = fmaf(xs, bf_lo(wv[d]), acc[2 * d]); acc[2 * d + 1] = fmaf(xs, bf_hi(wv[d]), acc[2 * d + 1]); }
}

struct Lds {
  float osm[8][2][8][16][4];
  float qs[2][1024];
  float red[8][256];
  float redq[8][128];
  float msm[8][8], lsm[8][8];
  float cmb[8][64];
  float fsl[8][128];
  float hv[64];
  float wsc[8];
  float zx[4][64];
  unsigned cnt[8];
};

__global__ void __launch_bounds__(NTHR, 2)
fused_decode_kernel(const unsigned short* __restrict__ x, const unsigned short* __restrict__ kc, const unsigned short* __restrict__ vc,
                    const unsigned short* __restrict__ wq, const unsigned short* __restrict__ wo, const unsigned short* __restrict__ wg,
                    const unsigned short* __restrict__ wu, const unsigned short* __restrict__ wd, unsigned char* ws, unsigned short* __restrict__ out) {
  __shared__ __attribute__((aligned(16))) Lds L;
  const int b = blockIdx.x, tid = threadIdx.x, lane = tid & 63;
  const int w = __builtin_amdgcn_readfirstlane(tid >> 6);
  const int grp = b >> 6, split = b & 63, kb = b & 7;
  const int c = lane & 15, g4 = lane >> 4, l8 = lane & 7, r8 = lane >> 3;
  const bool bit3 = (lane & 8) != 0;
  const bool ffn = b < NFFN;
  const int cq = (b >> 3) & 3, tau = (b & 7) | (((b >> 5) & 1) << 3), piece = (b & 7) * 8 + (b >> 5);
  const int ocol = 256 * cq + 4 * piece;
  const __amdgpu_buffer_rsrc_t rs = MKRS(ws, WS_END);
  const unsigned t0lo = (unsigned)__builtin_amdgcn_s_memrealtime();
  STAMP(0);
  const int row0 = 128 * kb + 16 * w;
  const __amdgpu_buffer_rsrc_t rwq = MKRS(wq + (size_t)row0 * 4096 + 128 * (b >> 3), 15 * 8192 + 256);
  const __amdgpu_buffer_rsrc_t rx = MKRS(x, 2048);
  const v4u xa = __builtin_amdgcn_raw_buffer_load_b128(rx, lane * 32, 0, 0), xb = __builtin_amdgcn_raw_buffer_load_b128(rx, lane * 32 + 16, 0, 0);
  unsigned short xr[4];
#pragma unroll
  for (int t = 0; t < 4; ++t) xr[t] = __builtin_amdgcn_raw_buffer_load_b16(rx, g4 * 2 + 8 * t, row0 * 2, 0);
  const unsigned short xres = __builtin_amdgcn_raw_buffer_load_b16(rx, g4 * 2, ocol * 2, 0);
  if (w == WQP) {
    ST_SC1((v4u{~0u, ~0u, ~0u, ~0u}), WS_A + b * A_STR + lane * 16);
    ST_SC1((v4u{~0u, ~0u, ~0u, ~0u}), WS_A + b * A_STR + 1024 + lane * 16);
  }
  const unsigned tag = __builtin_amdgcn_raw_buffer_load_b32(rs, WS_EPOCH, 0, AUX_SC1) + 1u;
  const size_t key0 = (size_t)grp * SK + (split * 8 + w) * 32;
  const __amdgpu_buffer_rsrc_t rk = MKRS(kc + key0 * DH, 3 * (4 << 20) + 8192), rv = MKRS(vc + key0 * DH, 3 * (4 << 20) + 8192);
  const int kvo = (4 * (c & 3) + ((c >> 2) & 1)) * 256 + (c >> 3) * 64 + g4 * 16, vvo = lane * 16;
#define LDK(i) LDNT(rk, kvo + (((i) >> 1) & 1) * 512 + ((i) & 1) * 128, ((i) >> 3) * (4 << 20) + (((i) >> 2) & 1) * 4096)
#define LDV(i) LDNT(rv, vvo + ((i) & 3) * 1024, ((i) >> 3) * (4 << 20) + (((i) >> 2) & 1) * 4096)
#define ISSUE_V(lo, hi) do { _Pragma("unroll") for (int i_ = (lo); i_ < (hi); i_ += 2) { PACE2(44 + i_); V[i_] = LDV(i_); V[i_ + 1] = LDV(i_ + 1); } SB(); } while (0)
  v4u K[32], V[32], wqv[4], gv[4], uv[4];
#define LDWQ(t) wqv[t] = LDNT(rwq, g4 * 8192 + c * 16, (t) * 32768)
  LDWQ(0); K[0] = LDK(0); SB(); LDWQ(1); K[1] = LDK(1); SB(); LDWQ(2); K[2] = LDK(2); SB(); LDWQ(3); K[3] = LDK(3); SB();
  if (tid < 8) L.cnt[tid] = 0u;
  SB();
  BARRIER();
  if (PRIO2 && w >= 4) __builtin_amdgcn_s_setprio(PRIO2);
  const int cbg = ffn ? ((b >> 3) ^ 1) : 0, cbu = ffn ? ((b >> 3) ^ 2) : 0, fsz = ffn ? 15 * FF * 2 + 256 : 0;
  const __amdgpu_buffer_rsrc_t rg = MKRS(wg + (size_t)row0 * FF + 128 * cbg, fsz), ru = MKRS(wu + (size_t)row0 * FF + 128 * cbu, fsz);
#define LDG(t) gv[t] = LDNT(rg, g4 * (FF * 2) + c * 16, (t) * (4 * FF * 2))
#define LDU(t) uv[t] = LDNT(ru, g4 * (FF * 2) + c * 16, (t) * (4 * FF * 2))
#define KK(n, i) do { PACE2(n); K[i] = LDK(i); K[(i) + 1] = LDK((i) + 1); SB(); } while (0)
#define GK(n, t, i) do { PACE2(n); LDG(t); K[i] = LDK(i); SB(); } while (0)
#define UK(n, t, i) do { PACE2(n); LDU(t); K[i] = LDK(i); SB(); } while (0)
  float xn[4];
  {
    float v[16];
#pragma unroll
    for (int i = 0; i < 4; ++i) { v[2 * i] = bf_lo(xa[i]); v[2 * i + 1] = bf_hi(xa[i]); v[8 + 2 * i] = bf_lo(xb[i]); v[9 + 2 * i] = bf_hi(xb[i]); }
    float s = 0.f;
#pragma unroll
    for (int i = 0; i < 16; ++i) s += v[i];
    const float mean = wave_sum(s) * (1.0f / D);
    float q2 = 0.f;
#pragma unroll
    for (int i = 0; i < 16; ++i) { const float d = v[i] - mean; q2 = fmaf(d, d, q2); }
    const float rstd = 1.0f / sqrtf(wave_sum(q2) * (1.0f / D) + 1e-5f);
#pragma unroll
    for (int t = 0; t < 4; ++t) xn[t] = (bf_lo(xr[t]) - mean) * rstd;
  }
  SB();
  KK(8, 4); KK(10, 6); GK(12, 0, 8);
  {
    float acc[8];
#pragma unroll
    for (int i = 0; i < 8; ++i) acc[i] = 0.f;
#pragma unroll
    for (int t = 0; t < 4; ++t) fma8(acc, xn[t], wqv[t]);
    *(f32x2*)&L.redq[w][8 * c + 2 * g4] = tr_red4(acc);
  }
  ARRIVE(0);
  STAMP(1);
  if (w == WQP) {
    WAITN(0, 8u);
    PACE(6);
    __builtin_amdgcn_s_setprio(3);
    if (lane < 32) {
      f32x4 s = {0.f, 0.f, 0.f, 0.f};
#pragma unroll
      for (int i = 0; i < 8; ++i) s += *(const f32x4*)&L.redq[i][4 * lane];
      const v4u g = {cvtpk(s[0], s[1]), tag, cvtpk(s[2], s[3]), tag};
      ST_SC1(g, WS_Q + ((grp * 8 + kb) * 512 + (split >> 3) * 64) * 8 + lane * 16);
    }
    if (PRIO2 && w >= 4) __builtin_amdgcn_s_setprio(PRIO2); else __builtin_amdgcn_s_setprio(0);
  }
  SB();
  KK(14, 9); UK(16, 0, 11); KK(18, 12); STAMP(11); GK(20, 1, 14); KK(22, 15); UK(24, 1, 17); STAMP(12);
  const int qoff = WS_Q + (grp * 8 + 4 * (tid >> 8)) * 4096 + (tid & 255) * 16;
#define QOK (qa[1] == tag && qa[3] == tag && qb[1] == tag && qb[3] == tag && qc[1] == tag && qc[3] == tag && qd[1] == tag && qd[3] == tag)
#define QLD() do { qa = LD_SC1(qoff); qb = LD_SC1(qoff + 4096); qc = LD_SC1(qoff + 8192); qd = LD_SC1(qoff + 12288); } while (0)
  PACEW();
  v4u qa, qb, qc, qd;
  QLD();
  SYNC(8); KK(26, 18); GK(28, 2, 20); KK(30, 21); UK(32, 2, 23); STAMP(13);
  if (__builtin_expect(!__all(QOK), 0)) { QLD(); }
  SB();
  KK(34, 24); GK(36, 3, 26); KK(38, 27); UK(40, 3, 29);
  POLL_WAIT(QOK, { QLD(); });
  {
    float* qs = &L.qs[0][0];
    const f32x4 s = {(bf_lo(qa[0]) + bf_lo(qb[0])) + (bf_lo(qc[0]) + bf_lo(qd[0])), (bf_hi(qa[0]) + bf_hi(qb[0])) + (bf_hi(qc[0]) + bf_hi(qd[0])),
                     (bf_lo(qa[2]) + bf_lo(qb[2])) + (bf_lo(qc[2]) + bf_lo(qd[2])), (bf_hi(qa[2]) + bf_hi(qb[2])) + (bf_hi(qc[2]) + bf_hi(qd[2]))};
    *(f32x4*)&qs[(tid >> 8) * 1024 + (tid & 255) * 4] = s;
  }
  ARRIVE(1);
  STAMP(2);
  KK(42, 30);
  ISSUE_V(0, 4);
  if (ffn) {
    float ag[8], au[8];
#pragma unroll
    for (int i = 0; i < 8; ++i) { ag[i] = 0.f; au[i] = 0.f; }
#pragma unroll
    for (int t = 0; t < 4; ++t) { fma8(ag, xn[t], gv[t]); fma8(au, xn[t], uv[t]); }
    *(f32x2*)&L.red[w][8 * c + 2 * g4] = tr_red4(ag);
    *(f32x2*)&L.red[w][128 + 8 * c + 2 * g4] = tr_red4(au);
    ARRIVE(3);
    if (w == WFP) {
      WAITN(3, 8u);
      f32x2 sg = {0.f, 0.f}, su = {0.f, 0.f};
#pragma unroll
      for (int i = 0; i < 8; ++i) { sg += *(const f32x2*)&L.red[i][2 * lane]; su += *(const f32x2*)&L.red[i][128 + 2 * lane]; }
      const v4u g0 = {f2u(sg[0]), tag, f2u(sg[1]), tag}, g1 = {f2u(su[0]), tag, f2u(su[1]), tag};
      ST_SC1(g0, WS_F + ((2 * cbg + (lane >> 5)) * 8 + kb) * 1024 + (lane & 31) * 16);
      ST_SC1(g1, WS_F + ((2 * cbu + (lane >> 5)) * 8 + kb) * 1024 + 512 + (lane & 31) * 16);
    }
  }
  SB();
  ISSUE_V(4, 8);
  WAITN(1, 8u);
  v4u Q[2];
  {
    const float* qs = &L.qs[0][0] + (c & 7) * DH + 32 * (c >> 3) + 8 * g4;
#pragma unroll
    for (int ln = 0; ln < 2; ++ln) {
      const f32x4 f0 = *(const f32x4*)(qs + 64 * ln) + *(const f32x4*)(qs + 1024 + 64 * ln);
      const f32x4 f1 = *(const f32x4*)(qs + 64 * ln + 4) + *(const f32x4*)(qs + 1024 + 64 * ln + 4);
      Q[ln] = v4u{cvtpk(f0[0], f0[1]), cvtpk(f0[2], f0[3]), cvtpk(f1[0], f1[1]), cvtpk(f1[2], f1[3])};
    }
  }
  constexpr float C2 = 0.08838834764831845f * 1.4426950408889634f;
  float S[8][4];
  float m = -INFINITY;
#pragma unroll
  for (int p = 0; p < 8; ++p) {
    f32x4 X = {0.f, 0.f, 0.f, 0.f}, Y = {0.f, 0.f, 0.f, 0.f};
    X = mfma(K[4 * p], Q[0], X); Y = mfma(K[4 * p + 2], Q[0], Y);
    X = mfma(K[4 * p + 1], Q[1], X); Y = mfma(K[4 * p + 3], Q[1], Y);
#pragma unroll
    for (int i = 0; i < 4; ++i) {
      auto sw = __builtin_amdgcn_permlane32_swap(f2u(X[i]), f2u(Y[i]), false, false);
      const float s = u2f(sw[0]) + dpp<XOR8>(u2f(sw[1]));
      S[p][i] = s; m = fmaxf(m, s);
    }
  }
  m = x16_max(m); m = x32_max(m);
  if (lane < 8) L.msm[lane][w] = m;
  ARRIVE(6);
  STAMP(3);
  ISSUE_V(8, 14);
  WAITN(6, 8u);
  float mc;
  {
    const f32x4 m0 = *(const f32x4*)&L.msm[c & 7][0], m1 = *(const f32x4*)&L.msm[c & 7][4];
    mc = fmaxf(fmaxf(fmaxf(m0[0], m0[1]), fmaxf(m0[2], m0[3])), fmaxf(fmaxf(m1[0], m1[1]), fmaxf(m1[2], m1[3]))) * C2;
  }
  float l = 0.f;
  v4u P[4];
#pragma unroll
  for (int u = 0; u < 4; ++u)
#pragma unroll
    for (int jj = 0; jj < 4; ++jj) {
      const int p = 2 * u + (jj >> 1), i0 = 2 * (jj & 1);
      const float p0 = __builtin_amdgcn_exp2f(fmaf(S[p][i0], C2, -mc)), p1 = __builtin_amdgcn_exp2f(fmaf(S[p][i0 + 1], C2, -mc));
      const unsigned pk = cvtpk(p0, p1);
      l += bf_lo(pk) + bf_hi(pk);
      P[u][jj] = pk;
    }
  l = x16_sum(l); l = x32_sum(l);
  if (lane < 8) L.lsm[lane][w] = l;
  ARRIVE(4);
  f32x4 O[8];
#pragma unroll
  for (int i = 0; i < 8; ++i) O[i] = f32x4{0.f, 0.f, 0.f, 0.f};
#define PV(u) do { _Pragma("unroll") for (int i = 0; i < 8; ++i) { v4u bfrag; \
      _Pragma("unroll") for (int jj = 0; jj < 4; ++jj) \
        bfrag[jj] = __builtin_amdgcn_perm(V[8 * (u) + 2 * jj + 1][i >> 1], V[8 * (u) + 2 * jj][i >> 1], (i & 1) ? 0x07060302u : 0x05040100u); \
      O[i] = mfma(P[u], bfrag, O[i]); } SB(); } while (0)
  SB();
  PV(0);
  ISSUE_V(14, 18);
  if (w == WMP) {
    WAITN(4, 8u);
    if (lane < 8) {
      const f32x4 l0 = *(const f32x4*)&L.lsm[lane][0], l1 = *(const f32x4*)&L.lsm[lane][4];
      const float Ls = ((l0[0] + l0[1]) + (l0[2] + l0[3])) + ((l1[0] + l1[1]) + (l1[2] + l1[3]));
      ST_SC1((v4u{f2u(mc), tag, f2u(Ls), tag}), WS_ML + b * 128 + lane * 16);
    }
  }
  SB();
  const int rbd = (b >> 5) * 8 + (b & 7);
  const __amdgpu_buffer_rsrc_t rf = MKRS(ws + WS_F + (ffn ? rbd * 8192 : 0) + w * 1024, ffn ? 1024 : 0);
  const unsigned ftag = ffn ? tag : 0u;
#define FLD() __builtin_amdgcn_raw_buffer_load_b128(rf, lane * 16, 0, AUX_SC1)
  v4u fv = FLD();
  ISSUE_V(18, 22);
  SYNC(1);
  PV(1);
  ISSUE_V(22, 26);
  STAMP(14);
  POLL_WAIT(fv[1] == ftag && fv[3] == ftag, { fv = FLD(); });
  *(f32x2*)&L.fsl[w][2 * lane] = f32x2{u2f(fv[0]), u2f(fv[2])};
  ARRIVE(5);
  SYNC(2);
  const int moff = WS_ML + (grp * 64 + lane) * 128 + w * 16;
  v4u mv = LD_SC1(moff);
  ISSUE_V(26, 30);
  PV(2);
  ISSUE_V(30, 32);
  SYNC(4);
  v4u wdv[4], wov[4];
  const int rh = w >> 2, cq4 = w & 3;
  const __amdgpu_buffer_rsrc_t rwd = MKRS(wd + (size_t)(ffn ? 64 * rbd + 32 * rh : 0) * D + 256 * cq + 64 * cq4, ffn ? 31 * 2048 + 128 : 0);
  const __amdgpu_buffer_rsrc_t rwo = MKRS(wo + (size_t)((8 * grp + 4 * (tau >> 3)) * 128 + 64 * (tau & 1) + 2 * ((tau >> 1) & 3) + 32 * rh) * D + 256 * cq + 64 * cq4, 409 * 2048 + 128);
  const int wdo = r8 * 8192 + l8 * 16, woo = ((r8 & 1) * 256 + 8 * (r8 >> 1)) * 2048 + l8 * 16;
  PACE2(76); wdv[0] = LDNT(rwd, wdo, 0); wdv[1] = LDNT(rwd, wdo + 2048, 0);
  SB();
  if (w == WFG) {
    WAITN(5, 8u);
    float gs = 0.f, us = 0.f;
#pragma unroll
    for (int i = 0; i < 8; ++i) { gs += L.fsl[i][lane]; us += L.fsl[i][64 + lane]; }
    L.hv[lane] = gelu_tanh(gs) * us;
  }
  SB();
  POLL_WAIT(mv[1] == tag && mv[3] == tag, { mv = LD_SC1(moff); });
  {
    const float ms = u2f(mv[0]);
    float M = lo3_max(ms); M = hi3_max(M);
    const float Lsum = wave_sum(u2f(mv[2]) * __builtin_amdgcn_exp2f(ms - M));
    const float mown = u2f(__builtin_amdgcn_readlane(mv[0], split));
    if (lane == 0) L.wsc[w] = __builtin_amdgcn_exp2f(mown - M) / Lsum;
  }
  SB();
  PACE2(78); wdv[2] = LDNT(rwd, wdo, 4096); wdv[3] = LDNT(rwd, wdo + 2048, 4096);
  SB();
  PV(3);
  STAMP(4);
  if (g4 < 2) {
#pragma unroll
    for (int i = 0; i < 8; ++i) *(f32x4*)&L.osm[w][g4][i][c][0] = O[i];
  }
  ARRIVE(2);
  wov[0] = LDNT(rwo, woo, 0); wov[1] = LDNT(rwo, woo + 2048, 0);
  wov[2] = LDNT(rwo, woo, 128 * 2048); wov[3] = LDNT(rwo, woo + 2048, 128 * 2048);
  SB();
  WAITN(2, 8u);
  {
    const float* src = &L.osm[0][w >> 2][2 * (w & 3)][0][0] + lane;
    const float sc = L.wsc[4 * (w >> 2) + (lane & 3)];
    float o0 = 0.f, o1 = 0.f;
#pragma unroll
    for (int i = 0; i < 8; ++i) { o0 += src[i * 1024]; o1 += src[i * 1024 + 64]; }
    unsigned pk = cvtpk(o0 * sc, o1 * sc);
    pk = pk < 0xFFFFFFFEu ? pk : 0xFFFFFFFEu;
    const v4u g = {pk, (unsigned)__builtin_amdgcn_mov_dpp((int)pk, 0x55, 0xf, 0xf, true), (unsigned)__builtin_amdgcn_mov_dpp((int)pk, 0xAA, 0xf, 0xf, true),
                   (unsigned)__builtin_amdgcn_mov_dpp((int)pk, 0xFF, 0xf, 0xf, true)};
    if ((lane & 3) == 0) ST_SC1(g, WS_A + b * A_STR + w * 256 + lane * 4);
  }
  SB();
  STAMP(5);
  float acc[8];
#pragma unroll
  for (int i = 0; i < 8; ++i) acc[i] = 0.f;
  if (ffn) {
    const f32x4 h = *(const f32x4*)&L.hv[32 * rh + 4 * r8];
#pragma unroll
    for (int t = 0; t < 4; ++t) fma8(acc, h[t], wdv[t]);
  }
  SB();
  { const unsigned td = (unsigned)__builtin_amdgcn_s_memrealtime(); while ((int)((unsigned)__builtin_amdgcn_s_memrealtime() - td) < ADELAY) { } }
  SB();
  const int aoff = WS_A + (grp * 64 + 8 * w + r8) * A_STR + tau * 128 + l8 * 16;
  v4u av = LD_SC1(aoff);
  STAMP(6);
  POLL_WAIT(av[0] != ~0u && av[1] != ~0u && av[2] != ~0u && av[3] != ~0u, { av = LD_SC1(aoff); });
  STAMP(7);
  {
    const float a[8] = {bf_lo(av[0]), bf_hi(av[0]), bf_lo(av[1]), bf_hi(av[1]), bf_lo(av[2]), bf_hi(av[2]), bf_lo(av[3]), bf_hi(av[3])};
    L.cmb[w][8 * l8 + r8] = tr_red8(a, bit3);
  }
  BARRIER();
  {
    const f32x4 ov = *(const f32x4*)&L.cmb[l8][32 * rh + 4 * r8];
#pragma unroll
    for (int t = 0; t < 4; ++t) fma8(acc, lo3_sum(ov[t]), wov[t]);
  }
  float z = tr_red8(acc, bit3);
  if (rh == 1) L.zx[cq4][lane] = z;
  BARRIER();
  if (rh == 0) {
    z += L.zx[cq4][lane];
    const float zn = dpp<XOR8>(z);
    if (!bit3) ST_SC1((v4u{f2u(z), tag, f2u(zn), tag}), WS_Z + b * Z_STR + (64 * cq4 + 8 * l8 + r8) * 8);
  }
  SB();
  STAMP(8);
  if (w == 7) {
    { const unsigned td = (unsigned)__builtin_amdgcn_s_memrealtime(); while ((int)((unsigned)__builtin_amdgcn_s_memrealtime() - td) < ZDELAY) { } }
    const int zoff = WS_Z + ((r8 << 5) | (cq << 3) | l8) * Z_STR + piece * 32;
    v4u za = LD_SC1(zoff), zb = LD_SC1(zoff + 16);
    POLL_WAIT(za[1] == tag && za[3] == tag && zb[1] == tag && zb[3] == tag, { za = LD_SC1(zoff); zb = LD_SC1(zoff + 16); });
    STAMP(9);
    float u = tr16(tr32(u2f(za[0]), u2f(zb[0])), tr32(u2f(za[2]), u2f(zb[2])));
    u += dpp<XOR1>(u); u += dpp<XOR2>(u); u += dpp<XOR7>(u); u += dpp<XOR8>(u);
    if (c == 0) out[ocol + g4] = (unsigned short)cvtpk(bf_lo(xres) + u, 0.f);
    if (b == 0 && lane == 0) __builtin_amdgcn_raw_buffer_store_b32(tag, rs, WS_EPOCH, 0, AUX_SC1);
    STAMP(10);
  }
}

extern "C" void kernel_launch(void* const* d_in, const int* in_sizes, int n_in, void* d_out, int out_size, void* d_ws, size_t ws_size,
                              hipStream_t stream) {
  fused_decode_kernel<<<NWG, NTHR, 0, stream>>>((const unsigned short*)d_in[0], (const unsigned short*)d_in[1], (const unsigned short*)d_in[2],
                                                (const unsigned short*)d_in[3], (const unsigned short*)d_in[4], (const unsigned short*)d_in[6],
                                                (const unsigned short*)d_in[7], (const unsigned short*)d_in[8], (unsigned char*)d_ws,
                                                (unsigned short*)d_out);
}
```

```cpp
#include <hip/hip_runtime.h>
#include <hip/hip_bf16.h>
#include <stdint.h>

typedef unsigned v4u __attribute__((ext_vector_type(4)));
typedef unsigned v2u __attribute__((ext_vector_type(2)));
typedef float f32x4 __attribute__((ext_vector_type(4)));
typedef float f32x2 __attribute__((ext_vector_type(2)));
typedef short bf16x8 __attribute__((ext_vector_type(8)));
typedef __bf16 bf16x2_t __attribute__((ext_vector_type(2)));

constexpr int D = 1024, SK = 65536, DH = 128, FF = 3584;
constexpr int NWG = 256, NTHR = 512, NFFN = 224;
constexpr int AUX_SC1 = 16, AUX_NT = 2;

constexpr int WS_EPOCH = 0;
constexpr int WS_Q = 4096;
constexpr int WS_Q2 = WS_Q + 131072;
constexpr int WS_F = WS_Q2 + 16384;
constexpr int WS_ML = WS_F + 56 * 8 * 512;
constexpr int A_STR = 2048 + 128, Z_STR = 1024 + 128;
constexpr int WS_A = WS_ML + 16384;
constexpr int WS_Z = WS_A + 256 * A_STR;
constexpr int WS_END = WS_Z + 256 * Z_STR;

constexpr int TAU16 = 465, T_OFF = 100, Z_DELAY = 30;
constexpr unsigned SPIN_CAP = 1u << 19;

__device__ __forceinline__ float bf_lo(unsigned u) { return __builtin_bit_cast(float, u << 16); }
__device__ __forceinline__ float bf_hi(unsigned u) { return __builtin_bit_cast(float, u & 0xffff0000u); }
__device__ __forceinline__ unsigned cvtpk(float lo, float hi) {
  return __builtin_bit_cast(unsigned, __builtin_convertvector(f32x2{lo, hi}, bf16x2_t));
}
__device__ __forceinline__ unsigned f2u(float f) { return __builtin_bit_cast(unsigned, f); }
__device__ __forceinline__ float u2f(unsigned u) { return __builtin_bit_cast(float, u); }
__device__ __forceinline__ f32x4 mfma(v4u a, v4u b, f32x4 c) {
  return __builtin_amdgcn_mfma_f32_16x16x32_bf16(__builtin_bit_cast(bf16x8, a), __builtin_bit_cast(bf16x8, b), c, 0, 0, 0);
}
template <int CTRL> __device__ __forceinline__ float dpp(float x) {
  return __builtin_bit_cast(float, __builtin_amdgcn_mov_dpp(__builtin_bit_cast(int, x), CTRL, 0xf, 0xf, true));
}
constexpr int XOR1 = 0xB1, XOR2 = 0x4E, XOR7 = 0x141, XOR8 = 0x128, ROR4 = 0x124;
__device__ __forceinline__ float x16_sum(float x) {
  auto s = __builtin_amdgcn_permlane16_swap(f2u(x), f2u(x), false, false);
  return u2f(s[0]) + u2f(s[1]);
}
__device__ __forceinline__ float x32_sum(float x) {
  auto s = __builtin_amdgcn_permlane32_swap(f2u(x), f2u(x), false, false);
  return u2f(s[0]) + u2f(s[1]);
}
__device__ __forceinline__ float x16_max(float x) {
  auto s = __builtin_amdgcn_permlane16_swap(f2u(x), f2u(x), false, false);
  return fmaxf(u2f(s[0]), u2f(s[1]));
}
__device__ __forceinline__ float x32_max(float x) {
  auto s = __builtin_amdgcn_permlane32_swap(f2u(x), f2u(x), false, false);
  return fmaxf(u2f(s[0]), u2f(s[1]));
}
__device__ __forceinline__ float wave_sum(float x) {
  x += dpp<XOR1>(x); x += dpp<XOR2>(x); x += dpp<XOR7>(x); x += dpp<XOR8>(x);
  x = x16_sum(x); x = x32_sum(x); return x;
}
__device__ __forceinline__ float wave_max(float x) {
  x = fmaxf(x, dpp<XOR1>(x)); x = fmaxf(x, dpp<XOR2>(x)); x = fmaxf(x, dpp<XOR7>(x)); x = fmaxf(x, dpp<XOR8>(x));
  x = x16_max(x); x = x32_max(x); return x;
}
__device__ __forceinline__ float tr32(float a, float b) {
  auto s = __builtin_amdgcn_permlane32_swap(f2u(a), f2u(b), false, false);
  return u2f(s[0]) + u2f(s[1]);
}
__device__ __forceinline__ float tr16(float a, float b) {
  auto s = __builtin_amdgcn_permlane16_swap(f2u(a), f2u(b), false, false);
  return u2f(s[0]) + u2f(s[1]);
}
__device__ __forceinline__ float tr8(float a, float b, bool bit3) {
  const float keep = bit3 ? b : a, send = bit3 ? a : b;
  return keep + dpp<XOR8>(send);
}
__device__ __forceinline__ float tr_red8(const float (&a)[8], bool bit3) {
  const float b0 = tr32(a[0], a[4]), b1 = tr32(a[1], a[5]), b2 = tr32(a[2], a[6]), b3 = tr32(a[3], a[7]);
  const float c0 = tr16(b0, b2), c1 = tr16(b1, b3);
  return tr8(c0, c1, bit3);
}
__device__ __forceinline__ f32x2 tr_red4(const float (&a)[8]) {
  const float b0 = tr32(a[0], a[4]), b1 = tr32(a[1], a[5]), b2 = tr32(a[2], a[6]), b3 = tr32(a[3], a[7]);
  return f32x2{tr16(b0, b2), tr16(b1, b3)};
}
__device__ __forceinline__ f32x4 red8to4(const float (&a)[8]) {
  f32x4 r;
#pragma unroll
  for (int i = 0; i < 4; ++i) { float t = tr32(a[i], a[4 + i]); t = x16_sum(t); t += dpp<XOR8>(t); r[i] = t; }
  return r;
}
__device__ __forceinline__ float gelu_tanh(float x) {
  const float u = 0.7978845608028654f * (x + 0.044715f * x * x * x);
  const float e = __expf(2.f * u);
  const float t = 1.f - 2.f / (e + 1.f);
  return 0.5f * x * (1.f + t);
}
__device__ __forceinline__ v4u pack24(f32x4 v, unsigned tag) {
  v4u r;
#pragma unroll
  for (int j = 0; j < 4; ++j) r[j] = __builtin_amdgcn_perm(f2u(v[j]) + 0x80u, tag, 0x07060500u + j);
  return r;
}
__device__ __forceinline__ bool ok24(v4u r, unsigned tag) {
  const unsigned a = __builtin_amdgcn_perm(r[1], r[0], 0x0c0c0400u), b = __builtin_amdgcn_perm(r[3], r[2], 0x04000c0cu);
  return (a | b) == tag;
}
__device__ __forceinline__ float val24(unsigned r) { return u2f(r & 0xffffff00u); }
__device__ __forceinline__ void fma8(float (&acc)[8], float xs, v4u wv) {
#pragma unroll
  for (int d = 0; d < 4; ++d) { acc[2 * d] = fmaf(xs, bf_lo(wv[d]), acc[2 * d]); acc[2 * d + 1] = fmaf(xs, bf_hi(wv[d]), acc[2 * d + 1]); }
}

#define SB() __builtin_amdgcn_sched_barrier(0)
#define STR2(x) #x
#define STR(x) STR2(x)
#define VMWAIT(n) do { SB(); asm volatile("s_waitcnt vmcnt(" STR(n) ")" ::: "memory"); SB(); } while (0)
#define BARRIER() do { asm volatile("s_waitcnt lgkmcnt(0)" ::: "memory"); __builtin_amdgcn_s_barrier(); asm volatile("" ::: "memory"); } while (0)
#define RESYNC() do { SB(); __builtin_amdgcn_s_barrier(); SB(); } while (0)
#define MKRS(p, n) __builtin_amdgcn_make_buffer_rsrc((void*)(p), 0, (n), 0x00020000)
#define LDNT(r, voff, soff) __builtin_amdgcn_raw_buffer_load_b128((r), (voff), (soff), AUX_NT)
#define LD_SC1(off) __builtin_amdgcn_raw_buffer_load_b128(rs, (off), 0, AUX_SC1)
#define ST_SC1(v, off) __builtin_amdgcn_raw_buffer_store_b128((v), rs, (off), 0, AUX_SC1)
#define NOW() ((unsigned)__builtin_amdgcn_s_memrealtime())
#define POLL_WAIT(cond, reload) do { if (__builtin_expect(!__all(cond), 0)) { unsigned spins_ = 0; \
    do { asm volatile("" ::: "memory"); if (!(cond)) { reload; } } while (!__all(cond) && ++spins_ < SPIN_CAP); } } while (0)
#define ARRIVE(k) do { asm volatile("s_waitcnt lgkmcnt(0)" ::: "memory"); \
    if (lane == 0) __hip_atomic_fetch_add(&L.cnt[k], 1u, __ATOMIC_RELAXED, __HIP_MEMORY_SCOPE_WORKGROUP); asm volatile("" ::: "memory"); } while (0)
#define WAITN(k, n) do { asm volatile("" ::: "memory"); \
    while (__hip_atomic_load(&L.cnt[k], __ATOMIC_RELAXED, __HIP_MEMORY_SCOPE_WORKGROUP) < (n)) { } asm volatile("" ::: "memory"); } while (0)
#define DUE(n) (T_OFF + (((n) - 6) * TAU16) / 16)
#define TGATE(n) do { SB(); while ((int)(NOW() - t0lo) < DUE(n)) { } SB(); } while (0)
#define RELAXW(n) (((n) >= 14 && (n) <= 24) ? 0 : ((n) >= 48 && (n) <= 56) ? 1 : ((n) >= 62 && (n) <= 68) ? 3 : -1)
#define PACE2(n) do { if (RELAXW(n) >= 0 && w == RELAXW(n)) { VMWAIT(12); } else { VMWAIT(4); } TGATE(n); } while (0)

struct Lds {
  float osm[8][2][8][16][4];
  unsigned qb[8][68];
  float red[8][256];
  float redq[8][128];
  float msm[8][8], lsm[8][8];
  float cmb[4][64];
  float fsl[8][128];
  float hv[64];
  float wsc[8];
  float zx[4][64];
  unsigned ln[4];
  unsigned cnt[16];
};

__global__ void __launch_bounds__(NTHR, 2)
fused_decode_kernel(const unsigned short* __restrict__ x, const unsigned short* __restrict__ kc, const unsigned short* __restrict__ vc,
                    const unsigned short* __restrict__ wq, const unsigned short* __restrict__ wo, const unsigned short* __restrict__ wg,
                    const unsigned short* __restrict__ wu, const unsigned short* __restrict__ wd, unsigned char* ws, unsigned short* __restrict__ out) {
  __shared__ __attribute__((aligned(16))) Lds L;
  const int b = blockIdx.x, tid = threadIdx.x, lane = tid & 63;
  const int w = __builtin_amdgcn_readfirstlane(tid >> 6);
  const int grp = b >> 6, split = b & 63, kb = b & 7;
  const int c = lane & 15, g4 = lane >> 4, l8 = lane & 7, r8 = lane >> 3;
  const bool bit3 = (lane & 8) != 0;
  const bool ffn = b < NFFN;
  const int cq = (b >> 3) & 3, tau = (b & 7) | (((b >> 5) & 1) << 3), piece = (b & 7) * 8 + (b >> 5);
  const int ocol = 256 * cq + 4 * piece;
  const __amdgpu_buffer_rsrc_t rs = MKRS(ws, WS_END);
  asm volatile("" :: "s"(x), "s"(kc), "s"(vc), "s"(wq), "s"(wo), "s"(wg), "s"(wu), "s"(wd), "s"(ws), "s"(out));
  const unsigned t0lo = NOW();
  const int row0 = 128 * kb + 16 * w;
  const __amdgpu_buffer_rsrc_t rwq = MKRS(wq + (size_t)row0 * 4096 + 128 * (b >> 3), 15 * 8192 + 256);
  const __amdgpu_buffer_rsrc_t rx = MKRS(x, 2048);
  v4u xa = {0u, 0u, 0u, 0u}, xb = {0u, 0u, 0u, 0u};
  unsigned short xres = 0;
  unsigned epoch = 0u;
  if (w == 0) {
    xa = __builtin_amdgcn_raw_buffer_load_b128(rx, lane * 32, 0, 0); xb = __builtin_amdgcn_raw_buffer_load_b128(rx, lane * 32 + 16, 0, 0);
    epoch = __builtin_amdgcn_raw_buffer_load_b32(rs, WS_EPOCH, 0, AUX_SC1);
    xres = __builtin_amdgcn_raw_buffer_load_b16(rx, g4 * 2, ocol * 2, 0);
    ST_SC1((v4u{~0u, ~0u, ~0u, ~0u}), WS_A + b * A_STR + lane * 16);
    ST_SC1((v4u{~0u, ~0u, ~0u, ~0u}), WS_A + b * A_STR + 1024 + lane * 16);
  }
  const v2u xr = __builtin_amdgcn_raw_buffer_load_b64(rx, g4 * 8, row0 * 2, 0);
  SB();
  const size_t key0 = (size_t)grp * SK + (split * 8 + w) * 32;
  const __amdgpu_buffer_rsrc_t rk = MKRS(kc + key0 * DH, 3 * (4 << 20) + 8192), rv = MKRS(vc + key0 * DH, 3 * (4 << 20) + 8192);
  const int kvo = (4 * (c & 3) + ((c >> 2) & 1)) * 256 + (c >> 3) * 64 + g4 * 16, vvo = lane * 16;
#define LDK(i) LDNT(rk, kvo + (((i) >> 1) & 1) * 512 + ((i) & 1) * 128, ((i) >> 3) * (4 << 20) + (((i) >> 2) & 1) * 4096)
#define LDV(i) LDNT(rv, vvo + ((i) & 3) * 1024, ((i) >> 3) * (4 << 20) + (((i) >> 2) & 1) * 4096)
#define ISSUE_V(lo, hi) do { _Pragma("unroll") for (int i_ = (lo); i_ < (hi); i_ += 2) { PACE2(44 + i_); V[i_] = LDV(i_); V[i_ + 1] = LDV(i_ + 1); } SB(); } while (0)
  v4u K[32], V[32], wqv[4], gv[4], uv[4];
#define LDWQ(t) wqv[t] = LDNT(rwq, g4 * 32768 + c * 16, (t) * 8192)
  LDWQ(0); K[0] = LDK(0); SB(); LDWQ(1); K[1] = LDK(1); SB(); LDWQ(2); K[2] = LDK(2); SB(); LDWQ(3); K[3] = LDK(3); SB();
  if (tid < 16) L.cnt[tid] = 0u;
  SB();
  BARRIER();
  const int cbg = ffn ? ((b >> 3) ^ 1) : 0, cbu = ffn ? ((b >> 3) ^ 2) : 0, fsz = ffn ? 15 * FF * 2 + 256 : 0;
  const __amdgpu_buffer_rsrc_t rg = MKRS(wg + (size_t)row0 * FF + 128 * cbg, fsz), ru = MKRS(wu + (size_t)row0 * FF + 128 * cbu, fsz);
#define LDG(t) gv[t] = LDNT(rg, g4 * (4 * FF * 2) + c * 16, (t) * (FF * 2))
#define LDU(t) uv[t] = LDNT(ru, g4 * (4 * FF * 2) + c * 16, (t) * (FF * 2))
#define KK(n, i) do { PACE2(n); K[i] = LDK(i); K[(i) + 1] = LDK((i) + 1); SB(); } while (0)
#define GK(n, t, i) do { PACE2(n); LDG(t); K[i] = LDK(i); SB(); } while (0)
#define UK(n, t, i) do { PACE2(n); LDU(t); K[i] = LDK(i); SB(); } while (0)
  if (w == 0) {
    float v[16];
#pragma unroll
    for (int i = 0; i < 4; ++i) { v[2 * i] = bf_lo(xa[i]); v[2 * i + 1] = bf_hi(xa[i]); v[8 + 2 * i] = bf_lo(xb[i]); v[9 + 2 * i] = bf_hi(xb[i]); }
    float s = 0.f;
#pragma unroll
    for (int i = 0; i < 16; ++i) s += v[i];
    const float mean = wave_sum(s) * (1.0f / D);
    float q2 = 0.f;
#pragma unroll
    for (int i = 0; i < 16; ++i) { const float d = v[i] - mean; q2 = fmaf(d, d, q2); }
    const float rstd = 1.0f / sqrtf(wave_sum(q2) * (1.0f / D) + 1e-5f);
    if (lane == 0) { L.ln[0] = f2u(mean); L.ln[1] = f2u(rstd); L.ln[2] = epoch + 0x01010101u; }
    ARRIVE(9);
  }
  SB();
  KK(8, 4); KK(10, 6); GK(12, 0, 8);
  WAITN(9, 1u);
  float xn[4];
  unsigned tag;
  {
    const v4u ln = *(const v4u*)&L.ln[0];
    const float mean = u2f(ln[0]), rstd = u2f(ln[1]);
    tag = ln[2];
    xn[0] = (bf_lo(xr[0]) - mean) * rstd; xn[1] = (bf_hi(xr[0]) - mean) * rstd; xn[2] = (bf_lo(xr[1]) - mean) * rstd; xn[3] = (bf_hi(xr[1]) - mean) * rstd;
  }
  {
    float acc[8];
#pragma unroll
    for (int i = 0; i < 8; ++i) acc[i] = 0.f;
#pragma unroll
    for (int t = 0; t < 4; ++t) fma8(acc, xn[t], wqv[t]);
    *(f32x2*)&L.redq[w][8 * c + 2 * g4] = tr_red4(acc);
  }
  ARRIVE(0);
  if (w == 0) {
    WAITN(0, 8u);
    VMWAIT(6);
    if (lane < 32) {
      f32x4 s = {0.f, 0.f, 0.f, 0.f};
#pragma unroll
      for (int i = 0; i < 8; ++i) s += *(const f32x4*)&L.redq[i][4 * lane];
      ST_SC1(pack24(s, tag), WS_Q + (grp * 8 + kb) * 4096 + (split >> 3) * 512 + lane * 16);
    }
  }
  SB();
  KK(14, 9); UK(16, 0, 11); KK(18, 12);
  const __amdgpu_buffer_rsrc_t rq1 = MKRS(ws + WS_Q + grp * 32768 + split * 64, w == 2 ? 7 * 4096 + 64 : 0);
#define Q1LD() __builtin_amdgcn_raw_buffer_load_b128(rq1, ((lane >> 2) & 7) * 4096 + (lane & 3) * 16, 0, AUX_SC1)
  v4u q1 = Q1LD();
  SB();
  GK(20, 1, 14); KK(22, 15); UK(24, 1, 17);
  if (w == 2 && !__all(ok24(q1, tag))) q1 = Q1LD();
  SB();
  KK(26, 18); GK(28, 2, 20);
  if (w == 2) {
    POLL_WAIT(ok24(q1, tag), { q1 = Q1LD(); });
    float v[4];
#pragma unroll
    for (int j = 0; j < 4; ++j) { float t = val24(q1[j]); t += dpp<ROR4>(t); t += dpp<XOR8>(t); v[j] = x16_sum(t); }
    if (lane < 4) ST_SC1((v4u{cvtpk(v[0], v[1]), tag, cvtpk(v[2], v[3]), tag}), WS_Q2 + grp * 4096 + (split * 4 + lane) * 16);
  }
  SB();
  KK(30, 21); UK(32, 2, 23); KK(34, 24); GK(36, 3, 26);
  const int qoff = WS_Q2 + grp * 4096 + w * 512 + (lane & 31) * 16;
#define QOK (qa[1] == tag && qa[3] == tag)
  v4u qa = LD_SC1(qoff);
  SB();
  KK(38, 27); UK(40, 3, 29); KK(42, 30);
  if (__builtin_expect(!__all(QOK), 0)) qa = LD_SC1(qoff);
  SB();
  ISSUE_V(0, 4);
  if (ffn) {
    float ag[8], au[8];
#pragma unroll
    for (int i = 0; i < 8; ++i) { ag[i] = 0.f; au[i] = 0.f; }
#pragma unroll
    for (int t = 0; t < 4; ++t) { fma8(ag, xn[t], gv[t]); fma8(au, xn[t], uv[t]); }
    *(f32x2*)&L.red[w][8 * c + 2 * g4] = tr_red4(ag);
    *(f32x2*)&L.red[w][128 + 8 * c + 2 * g4] = tr_red4(au);
    ARRIVE(3);
    if (w == 1) {
      WAITN(3, 8u);
      f32x4 sg = {0.f, 0.f, 0.f, 0.f};
#pragma unroll
      for (int i = 0; i < 8; ++i) sg += *(const f32x4*)&L.red[i][4 * lane];
      const int cb = lane < 32 ? cbg : cbu;
      ST_SC1(pack24(sg, tag), WS_F + ((2 * cb + ((lane >> 4) & 1)) * 8 + kb) * 512 + (lane >> 5) * 256 + c * 16);
    }
  }
  SB();
  ISSUE_V(4, 6);
  POLL_WAIT(QOK, { qa = LD_SC1(qoff); });
  if (lane < 32) *(v2u*)&L.qb[w][2 * lane] = v2u{qa[0], qa[2]};
  ARRIVE(1);
  ISSUE_V(6, 8);
  WAITN(1, 8u);
  v4u Q[2];
  Q[0] = *(const v4u*)&L.qb[c & 7][16 * (c >> 3) + 4 * g4];
  Q[1] = *(const v4u*)&L.qb[c & 7][32 + 16 * (c >> 3) + 4 * g4];
  constexpr float C2 = 0.08838834764831845f * 1.4426950408889634f;
  float S[8][4];
  float m = -INFINITY;
#pragma unroll
  for (int p = 0; p < 8; ++p) {
    f32x4 X = {0.f, 0.f, 0.f, 0.f}, Y = {0.f, 0.f, 0.f, 0.f};
    X = mfma(K[4 * p], Q[0], X); Y = mfma(K[4 * p + 2], Q[0], Y);
    X = mfma(K[4 * p + 1], Q[1], X); Y = mfma(K[4 * p + 3], Q[1], Y);
#pragma unroll
    for (int i = 0; i < 4; ++i) {
      auto sw = __builtin_amdgcn_permlane32_swap(f2u(X[i]), f2u(Y[i]), false, false);
      const float s = u2f(sw[0]) + dpp<XOR8>(u2f(sw[1]));
      S[p][i] = s; m = fmaxf(m, s);
    }
  }
  m = x16_max(m); m = x32_max(m);
  if (lane < 8) L.msm[lane][w] = m;
  ARRIVE(6);
  ISSUE_V(8, 14);
  WAITN(6, 8u);
  float mc;
  {
    const f32x4 m0 = *(const f32x4*)&L.msm[c & 7][0], m1 = *(const f32x4*)&L.msm[c & 7][4];
    mc = fmaxf(fmaxf(fmaxf(m0[0], m0[1]), fmaxf(m0[2], m0[3])), fmaxf(fmaxf(m1[0], m1[1]), fmaxf(m1[2], m1[3]))) * C2;
  }
  float l = 0.f;
  v4u P[4];
#pragma unroll
  for (int u = 0; u < 4; ++u)
#pragma unroll
    for (int jj = 0; jj < 4; ++jj) {
      const int p = 2 * u + (jj >> 1), i0 = 2 * (jj & 1);
      const float p0 = __builtin_amdgcn_exp2f(fmaf(S[p][i0], C2, -mc)), p1 = __builtin_amdgcn_exp2f(fmaf(S[p][i0 + 1], C2, -mc));
      const unsigned pk = cvtpk(p0, p1);
      l += bf_lo(pk) + bf_hi(pk);
      P[u][jj] = pk;
    }
  l = x16_sum(l); l = x32_sum(l);
  if (lane < 8) L.lsm[lane][w] = l;
  ARRIVE(4);
  f32x4 O[8];
#pragma unroll
  for (int i = 0; i < 8; ++i) O[i] = f32x4{0.f, 0.f, 0.f, 0.f};
#define PV(u) do { _Pragma("unroll") for (int i = 0; i < 8; ++i) { v4u bfrag; \
      _Pragma("unroll") for (int jj = 0; jj < 4; ++jj) \
        bfrag[jj] = __builtin_amdgcn_perm(V[8 * (u) + 2 * jj + 1][i >> 1], V[8 * (u) + 2 * jj][i >> 1], (i & 1) ? 0x07060302u : 0x05040100u); \
      O[i] = mfma(P[u], bfrag, O[i]); } SB(); } while (0)
  SB();
  PV(0);
  ISSUE_V(14, 18);
  if (w == 3) {
    WAITN(4, 8u);
    if (lane < 8) {
      const f32x4 l0 = *(const f32x4*)&L.lsm[lane][0], l1 = *(const f32x4*)&L.lsm[lane][4];
      const float Ls = ((l0[0] + l0[1]) + (l0[2] + l0[3])) + ((l1[0] + l1[1]) + (l1[2] + l1[3]));
      __builtin_amdgcn_raw_buffer_store_b64(v2u{f2u(mc + __builtin_amdgcn_logf(Ls)), tag}, rs, WS_ML + ((grp * 8 + lane) * 64 + split) * 8, 0, AUX_SC1);
    }
  }
  SB();
  const int rbd = (b >> 5) * 8 + (b & 7);
  const __amdgpu_buffer_rsrc_t rf = MKRS(ws + WS_F + (ffn ? rbd * 4096 : 0) + w * 512, ffn ? 512 : 0);
  const unsigned ftag = ffn ? tag : 0u;
#define FLD() __builtin_amdgcn_raw_buffer_load_b128(rf, (lane & 31) * 16, 0, AUX_SC1)
  v4u fv = FLD();
  ISSUE_V(18, 22);
  RESYNC();
  PV(1);
  ISSUE_V(22, 26);
  POLL_WAIT(ok24(fv, ftag), { fv = FLD(); });
  if (lane < 32) *(f32x4*)&L.fsl[w][4 * lane] = f32x4{val24(fv[0]), val24(fv[1]), val24(fv[2]), val24(fv[3])};
  ARRIVE(5);
  RESYNC();
  const int moff = WS_ML + ((grp * 8 + w) * 64 + lane) * 8;
#define MLD() __builtin_amdgcn_raw_buffer_load_b64(rs, moff, 0, AUX_SC1)
  v2u mv = MLD();
  ISSUE_V(26, 30);
  PV(2);
  if (w == 2) {
    WAITN(5, 8u);
    float gs = 0.f, us = 0.f;
#pragma unroll
    for (int i = 0; i < 8; ++i) { gs += L.fsl[i][lane]; us += L.fsl[i][64 + lane]; }
    L.hv[lane] = gelu_tanh(gs) * us;
  }
  SB();
  ISSUE_V(30, 32);
  const int cq4 = w & 3;
  const int wvo = r8 * (8 * 2048) + l8 * 16;
  v4u wv[8];
#define ML_FINISH() do { \
  POLL_WAIT(mv[1] == tag, { mv = MLD(); }); \
  { const float ms = u2f(mv[0]); const float M = wave_max(ms); \
    const float Lsum = wave_sum(__builtin_amdgcn_exp2f(ms - M)); \
    if (lane == 0) L.wsc[w] = __builtin_amdgcn_exp2f(u2f(__builtin_amdgcn_readlane(f2u(mc), w)) - M) * __builtin_amdgcn_rcpf(Lsum); } SB(); } while (0)
#define FINISH_ATTN() do { \
  PV(3); \
  if (g4 < 2) { _Pragma("unroll") for (int i = 0; i < 8; ++i) *(f32x4*)&L.osm[w][g4][i][c][0] = O[i]; } \
  ARRIVE(2); \
  WAITN(2, 8u); \
  { const float* src = &L.osm[0][w >> 2][2 * (w & 3)][0][0] + lane; \
    const float sc = L.wsc[4 * (w >> 2) + (lane & 3)]; \
    float o0 = 0.f, o1 = 0.f; \
    _Pragma("unroll") for (int i = 0; i < 8; ++i) { o0 += src[i * 1024]; o1 += src[i * 1024 + 64]; } \
    unsigned pk = cvtpk(o0 * sc, o1 * sc); \
    pk = pk < 0xFFFFFFFEu ? pk : 0xFFFFFFFEu;     \
    const v4u g = {pk, (unsigned)__builtin_amdgcn_mov_dpp((int)pk, 0x55, 0xf, 0xf, true), (unsigned)__builtin_amdgcn_mov_dpp((int)pk, 0xAA, 0xf, 0xf, true), \
                   (unsigned)__builtin_amdgcn_mov_dpp((int)pk, 0xFF, 0xf, 0xf, true)}; \
    if ((lane & 3) == 0) ST_SC1(g, WS_A + b * A_STR + w * 256 + lane * 4); } \
  SB(); } while (0)
  if (w < 4) {
    const __amdgpu_buffer_rsrc_t rwd = MKRS(wd + (size_t)(ffn ? 64 * rbd : 0) * D + 256 * cq + 64 * cq4, ffn ? 63 * 2048 + 128 : 0);
    ML_FINISH();
    TGATE(76);
#pragma unroll
    for (int t = 0; t < 4; ++t) wv[t] = LDNT(rwd, wvo + (t & 1) * 2048, (t >> 1) * 4096);
    SB();
    FINISH_ATTN();
#pragma unroll
    for (int t = 4; t < 8; ++t) wv[t] = LDNT(rwd, wvo + (t & 1) * 2048, (t >> 1) * 4096);
    SB();
    {
      float acc[8];
#pragma unroll
      for (int i = 0; i < 8; ++i) acc[i] = 0.f;
      if (ffn) {
        const f32x4 h0 = *(const f32x4*)&L.hv[8 * r8], h1 = *(const f32x4*)&L.hv[8 * r8 + 4];
#pragma unroll
        for (int t = 0; t < 4; ++t) { fma8(acc, h0[t], wv[t]); fma8(acc, h1[t], wv[4 + t]); }
      }
      const f32x4 zd = red8to4(acc);
      if ((lane & 24) == 0) *(f32x4*)&L.zx[cq4][8 * l8 + 4 * (lane >> 5)] = zd;
    }
    SB();
    const int aoff = WS_A + (grp * 64 + 16 * w + r8) * A_STR + tau * 128 + l8 * 16;
#define AOK (av[0] != ~0u && av[1] != ~0u && av[2] != ~0u && av[3] != ~0u && aw[0] != ~0u && aw[1] != ~0u && aw[2] != ~0u && aw[3] != ~0u)
    v4u av = LD_SC1(aoff), aw = LD_SC1(aoff + 8 * A_STR);
    POLL_WAIT(AOK, { av = LD_SC1(aoff); aw = LD_SC1(aoff + 8 * A_STR); });
    {
      const float a[8] = {bf_lo(av[0]) + bf_lo(aw[0]), bf_hi(av[0]) + bf_hi(aw[0]), bf_lo(av[1]) + bf_lo(aw[1]), bf_hi(av[1]) + bf_hi(aw[1]),
                          bf_lo(av[2]) + bf_lo(aw[2]), bf_hi(av[2]) + bf_hi(aw[2]), bf_lo(av[3]) + bf_lo(aw[3]), bf_hi(av[3]) + bf_hi(aw[3])};
      L.cmb[w][8 * l8 + r8] = tr_red8(a, bit3);
    }
    ARRIVE(7);
    if (w == 0) {
      WAITN(8, 4u);
      { const unsigned td = NOW(); while ((int)(NOW() - td) < Z_DELAY) { } }
      const int zoff = WS_Z + ((r8 << 5) | (cq << 3) | l8) * Z_STR + piece * 16;
      v4u za = LD_SC1(zoff);
      POLL_WAIT(ok24(za, tag), { za = LD_SC1(zoff); });
      float u = tr16(tr32(val24(za[0]), val24(za[2])), tr32(val24(za[1]), val24(za[3])));
      u += dpp<XOR1>(u); u += dpp<XOR2>(u); u += dpp<XOR7>(u); u += dpp<XOR8>(u);
      if (c == 0) out[ocol + g4] = (unsigned short)cvtpk(bf_lo(xres) + u, 0.f);
      if (b == 0 && lane == 0) __builtin_amdgcn_raw_buffer_store_b32(tag, rs, WS_EPOCH, 0, AUX_SC1);
    }
  } else {
    const __amdgpu_buffer_rsrc_t rwo = MKRS(wo + (size_t)((8 * grp + 4 * (tau >> 3)) * 128 + 64 * (tau & 1) + 2 * ((tau >> 1) & 3)) * D + 256 * cq + 64 * cq4, 441 * 2048 + 128);
    ML_FINISH();
    FINISH_ATTN();
#pragma unroll
    for (int t = 0; t < 8; ++t) wv[t] = LDNT(rwo, wvo + (t & 1) * 2048, (t >> 1) * (128 * 2048));
    SB();
    WAITN(7, 4u);
    float acc[8];
#pragma unroll
    for (int i = 0; i < 8; ++i) acc[i] = 0.f;
    const f32x4 ov = *(const f32x4*)&L.cmb[l8 & 3][8 * r8 + (l8 & 4)];
    const f32x4 zd = *(const f32x4*)&L.zx[cq4][8 * l8 + 4 * (lane >> 5)];
    float oa[4], ob[4];
#pragma unroll
    for (int t = 0; t < 4; ++t) { float v = ov[t]; v += dpp<XOR1>(v); v += dpp<XOR2>(v); const float o = dpp<XOR7>(v); oa[t] = (l8 & 4) ? o : v; ob[t] = (l8 & 4) ? v : o; }
#pragma unroll
    for (int t = 0; t < 4; ++t) { fma8(acc, oa[t], wv[t]); fma8(acc, ob[t], wv[4 + t]); }
    const f32x4 z = red8to4(acc) + zd;
    if ((lane & 24) == 0) ST_SC1(pack24(z, tag), WS_Z + b * Z_STR + (64 * cq4 + 8 * l8 + 4 * (lane >> 5)) * 4);
    ARRIVE(8);
  }
}

extern "C" void kernel_launch(void* const* d_in, const int* in_sizes, int n_in, void* d_out, int out_size, void* d_ws, size_t ws_size,
                              hipStream_t stream) {
  fused_decode_kernel<<<NWG, NTHR, 0, stream>>>((const unsigned short*)d_in[0], (const unsigned short*)d_in[1], (const unsigned short*)d_in[2],
                                                (const unsigned short*)d_in[3], (const unsigned short*)d_in[4], (const unsigned short*)d_in[6],
                                                (const unsigned short*)d_in[7], (const unsigned short*)d_in[8], (unsigned char*)d_ws,
                                                (unsigned short*)d_out);
}
```

```cpp
#include <hip/hip_runtime.h>
#include <hip/hip_bf16.h>
#include <stdint.h>

typedef unsigned v4u __attribute__((ext_vector_type(4)));
typedef unsigned v2u __attribute__((ext_vector_type(2)));
typedef float f32x4 __attribute__((ext_vector_type(4)));
typedef float f32x2 __attribute__((ext_vector_type(2)));
typedef short bf16x8 __attribute__((ext_vector_type(8)));
typedef __bf16 bf16x2_t __attribute__((ext_vector_type(2)));

constexpr int D = 1024, SK = 65536, DH = 128, FF = 3584;
constexpr int NWG = 256, NTHR = 512, NFFN = 224;
constexpr int AUX_SC1 = 16, AUX_NT = 2;

constexpr int WS_EPOCH = 0;
constexpr int WS_Q = 4096;
constexpr int WS_Q2 = WS_Q + 131072;
constexpr int WS_F = WS_Q2 + 16384;
constexpr int WS_ML = WS_F + 56 * 8 * 512;
constexpr int A_STR = 2048 + 128, Z_STR = 1024 + 128;
constexpr int WS_A = WS_ML + 16384;
constexpr int WS_Z = WS_A + 256 * A_STR;
constexpr int WS_END = WS_Z + 256 * Z_STR;

constexpr int TAU16 = 465, T_OFF = 100, Z_DELAY = 30;
constexpr unsigned SPIN_CAP = 1u << 19;

__device__ __forceinline__ float bf_lo(unsigned u) { return __builtin_bit_cast(float, u << 16); }
__device__ __forceinline__ float bf_hi(unsigned u) { return __builtin_bit_cast(float, u & 0xffff0000u); }
__device__ __forceinline__ unsigned cvtpk(float lo, float hi) {
  return __builtin_bit_cast(unsigned, __builtin_convertvector(f32x2{lo, hi}, bf16x2_t));
}
__device__ __forceinline__ unsigned f2u(float f) { return __builtin_bit_cast(unsigned, f); }
__device__ __forceinline__ float u2f(unsigned u) { return __builtin_bit_cast(float, u); }
__device__ __forceinline__ f32x4 mfma(v4u a, v4u b, f32x4 c) {
  return __builtin_amdgcn_mfma_f32_16x16x32_bf16(__builtin_bit_cast(bf16x8, a), __builtin_bit_cast(bf16x8, b), c, 0, 0, 0);
}
template <int CTRL> __device__ __forceinline__ float dpp(float x) {
  return __builtin_bit_cast(float, __builtin_amdgcn_mov_dpp(__builtin_bit_cast(int, x), CTRL, 0xf, 0xf, true));
}
constexpr int XOR1 = 0xB1, XOR2 = 0x4E, XOR7 = 0x141, XOR8 = 0x128, ROR4 = 0x124;
__device__ __forceinline__ float x16_sum(float x) {
  auto s = __builtin_amdgcn_permlane16_swap(f2u(x), f2u(x), false, false);
  return u2f(s[0]) + u2f(s[1]);
}
__device__ __forceinline__ float x32_sum(float x) {
  auto s = __builtin_amdgcn_permlane32_swap(f2u(x), f2u(x), false, false);
  return u2f(s[0]) + u2f(s[1]);
}
__device__ __forceinline__ float x16_max(float x) {
  auto s = __builtin_amdgcn_permlane16_swap(f2u(x), f2u(x), false, false);
  return fmaxf(u2f(s[0]), u2f(s[1]));
}
__device__ __forceinline__ float x32_max(float x) {
  auto s = __builtin_amdgcn_permlane32_swap(f2u(x), f2u(x), false, false);
  return fmaxf(u2f(s[0]), u2f(s[1]));
}
__device__ __forceinline__ float wave_sum(float x) {
  x += dpp<XOR1>(x); x += dpp<XOR2>(x); x += dpp<XOR7>(x); x += dpp<XOR8>(x);
  x = x16_sum(x); x = x32_sum(x); return x;
}
__device__ __forceinline__ float wave_max(float x) {
  x = fmaxf(x, dpp<XOR1>(x)); x = fmaxf(x, dpp<XOR2>(x)); x = fmaxf(x, dpp<XOR7>(x)); x = fmaxf(x, dpp<XOR8>(x));
  x = x16_max(x); x = x32_max(x); return x;
}
__device__ __forceinline__ float tr32(float a, float b) {
  auto s = __builtin_amdgcn_permlane32_swap(f2u(a), f2u(b), false, false);
  return u2f(s[0]) + u2f(s[1]);
}
__device__ __forceinline__ float tr16(float a, float b) {
  auto s = __builtin_amdgcn_permlane16_swap(f2u(a), f2u(b), false, false);
  return u2f(s[0]) + u2f(s[1]);
}
__device__ __forceinline__ float tr8(float a, float b, bool bit3) {
  const float keep = bit3 ? b : a, send = bit3 ? a : b;
  return keep + dpp<XOR8>(send);
}
__device__ __forceinline__ float tr_red8(const float (&a)[8], bool bit3) {
  const float b0 = tr32(a[0], a[4]), b1 = tr32(a[1], a[5]), b2 = tr32(a[2], a[6]), b3 = tr32(a[3], a[7]);
  const float c0 = tr16(b0, b2), c1 = tr16(b1, b3);
  return tr8(c0, c1, bit3);
}
__device__ __forceinline__ f32x2 tr_red4(const float (&a)[8]) {
  const float b0 = tr32(a[0], a[4]), b1 = tr32(a[1], a[5]), b2 = tr32(a[2], a[6]), b3 = tr32(a[3], a[7]);
  return f32x2{tr16(b0, b2), tr16(b1, b3)};
}
__device__ __forceinline__ f32x4 red8to4(const float (&a)[8]) {
  f32x4 r;
#pragma unroll
  for (int i = 0; i < 4; ++i) { float t = tr32(a[i], a[4 + i]); t = x16_sum(t); t += dpp<XOR8>(t); r[i] = t; }
  return r;
}
__device__ __forceinline__ float gelu_tanh(float x) {
  const float u = 0.7978845608028654f * (x + 0.044715f * x * x * x);
  const float e = __expf(2.f * u);
  const float t = 1.f - 2.f / (e + 1.f);
  return 0.5f * x * (1.f + t);
}
__device__ __forceinline__ v4u pack24(f32x4 v, unsigned tag) {
  v4u r;
#pragma unroll
  for (int j = 0; j < 4; ++j) r[j] = __builtin_amdgcn_perm(f2u(v[j]) + 0x80u, tag, 0x07060500u + j);
  return r;
}
__device__ __forceinline__ bool ok24(v4u r, unsigned tag) {
  const unsigned a = __builtin_amdgcn_perm(r[1], r[0], 0x0c0c0400u), b = __builtin_amdgcn_perm(r[3], r[2], 0x04000c0cu);
  return (a | b) == tag;
}
__device__ __forceinline__ float val24(unsigned r) { return u2f(r & 0xffffff00u); }
__device__ __forceinline__ void fma8(float (&acc)[8], float xs, v4u wv) {
#pragma unroll
  for (int d = 0; d < 4; ++d) { acc[2 * d] = fmaf(xs, bf_lo(wv[d]), acc[2 * d]); acc[2 * d + 1] = fmaf(xs, bf_hi(wv[d]), acc[2 * d + 1]); }
}

#define SB() __builtin_amdgcn_sched_barrier(0)
#define STR2(x) #x
#define STR(x) STR2(x)
#define VMWAIT(n) do { SB(); asm volatile("s_waitcnt vmcnt(" STR(n) ")" ::: "memory"); SB(); } while (0)
#define BARRIER() do { asm volatile("s_waitcnt lgkmcnt(0)" ::: "memory"); __builtin_amdgcn_s_barrier(); asm volatile("" ::: "memory"); } while (0)
#define RESYNC() do { SB(); __builtin_amdgcn_s_barrier(); SB(); } while (0)
#define MKRS(p, n) __builtin_amdgcn_make_buffer_rsrc((void*)(p), 0, (n), 0x00020000)
#define LDNT(r, voff, soff) __builtin_amdgcn_raw_buffer_load_b128((r), (voff), (soff), AUX_NT)
#define LD_SC1(off) __builtin_amdgcn_raw_buffer_load_b128(rs, (off), 0, AUX_SC1)
#define ST_SC1(v, off) __builtin_amdgcn_raw_buffer_store_b128((v), rs, (off), 0, AUX_SC1)
#define NOW() ((unsigned)__builtin_amdgcn_s_memrealtime())
#define POLL_WAIT(cond, reload) do { if (__builtin_expect(!__all(cond), 0)) { unsigned spins_ = 0; \
    do { asm volatile("" ::: "memory"); if (!(cond)) { reload; } } while (!__all(cond) && ++spins_ < SPIN_CAP); } } while (0)
#define ARRIVE(k) do { asm volatile("s_waitcnt lgkmcnt(0)" ::: "memory"); \
    if (lane == 0) __hip_atomic_fetch_add(&L.cnt[k], 1u, __ATOMIC_RELAXED, __HIP_MEMORY_SCOPE_WORKGROUP); asm volatile("" ::: "memory"); } while (0)
#define WAITN(k, n) do { asm volatile("" ::: "memory"); \
    while (__hip_atomic_load(&L.cnt[k], __ATOMIC_RELAXED, __HIP_MEMORY_SCOPE_WORKGROUP) < (n)) { } asm volatile("" ::: "memory"); } while (0)
#define DUE(n) (T_OFF + (((n) - 6) * TAU16) / 16)
#define TGATE(n) do { SB(); while ((int)(NOW() - t0lo) < DUE(n)) { } SB(); } while (0)
#define RELAXW(n) (((n) >= 14 && (n) <= 24) ? 0 : ((n) >= 48 && (n) <= 56) ? 1 : ((n) >= 62 && (n) <= 68) ? 3 : -1)
#define PACE2(n) do { if (RELAXW(n) >= 0 && w == RELAXW(n)) { VMWAIT(12); } else { VMWAIT(4); } TGATE(n); } while (0)

struct Lds {
  float osm[8][2][8][16][4];
  unsigned qb[8][68];
  float red[8][256];
  float redq[8][128];
  float msm[8][8], lsm[8][8];
  float cmb[4][64];
  float fsl[8][128];
  float hv[64];
  float wsc[8];
  float zx[4][64];
  unsigned ln[4];
  unsigned cnt[16];
};

__global__ void __launch_bounds__(NTHR, 2)
fused_decode_kernel(const unsigned short* __restrict__ x, const unsigned short* __restrict__ kc, const unsigned short* __restrict__ vc,
                    const unsigned short* __restrict__ wq, const unsigned short* __restrict__ wo, const unsigned short* __restrict__ wg,
                    const unsigned short* __restrict__ wu, const unsigned short* __restrict__ wd, unsigned char* ws, unsigned short* __restrict__ out) {
  __shared__ __attribute__((aligned(16))) Lds L;
  const int b = blockIdx.x, tid = threadIdx.x, lane = tid & 63;
  const int w = __builtin_amdgcn_readfirstlane(tid >> 6);
  const int grp = b >> 6, split = b & 63, kb = b & 7;
  const int c = lane & 15, g4 = lane >> 4, l8 = lane & 7, r8 = lane >> 3;
  const bool bit3 = (lane & 8) != 0;
  const bool ffn = b < NFFN;
  const int cq = (b >> 3) & 3, tau = (b & 7) | (((b >> 5) & 1) << 3), piece = (b & 7) * 8 + (b >> 5);
  const int ocol = 256 * cq + 4 * piece;
  const __amdgpu_buffer_rsrc_t rs = MKRS(ws, WS_END);
  asm volatile("" :: "s"(x), "s"(kc), "s"(vc), "s"(wq), "s"(wo), "s"(wg), "s"(wu), "s"(wd), "s"(ws), "s"(out));
  const unsigned t0lo = NOW();
  const int row0 = 128 * kb + 16 * w;
  const __amdgpu_buffer_rsrc_t rwq = MKRS(wq + (size_t)row0 * 4096 + 128 * (b >> 3), 15 * 8192 + 256);
  const __amdgpu_buffer_rsrc_t rx = MKRS(x, 2048);
  v4u xa = {0u, 0u, 0u, 0u}, xb = {0u, 0u, 0u, 0u};
  unsigned short xres = 0;
  unsigned epoch = 0u;
  if (w == 0) {
    xa = __builtin_amdgcn_raw_buffer_load_b128(rx, lane * 32, 0, 0); xb = __builtin_amdgcn_raw_buffer_load_b128(rx, lane * 32 + 16, 0, 0);
    epoch = __builtin_amdgcn_raw_buffer_load_b32(rs, WS_EPOCH, 0, AUX_SC1);
    xres = __builtin_amdgcn_raw_buffer_load_b16(rx, g4 * 2, ocol * 2, 0);
    ST_SC1((v4u{~0u, ~0u, ~0u, ~0u}), WS_A + b * A_STR + lane * 16);
    ST_SC1((v4u{~0u, ~0u, ~0u, ~0u}), WS_A + b * A_STR + 1024 + lane * 16);
  }
  const v2u xr = __builtin_amdgcn_raw_buffer_load_b64(rx, g4 * 8, row0 * 2, 0);
  SB();
  const size_t key0 = (size_t)grp * SK + (split * 8 + w) * 32;
  const __amdgpu_buffer_rsrc_t rk = MKRS(kc + key0 * DH, 3 * (4 << 20) + 8192), rv = MKRS(vc + key0 * DH, 3 * (4 << 20) + 8192);
  const int kvo = (4 * (c & 3) + ((c >> 2) & 1)) * 256 + (c >> 3) * 64 + g4 * 16, vvo = lane * 16;
#define LDK(i) LDNT(rk, kvo + (((i) >> 1) & 1) * 512 + ((i) & 1) * 128, ((i) >> 3) * (4 << 20) + (((i) >> 2) & 1) * 4096)
#define LDV(i) LDNT(rv, vvo + ((i) & 3) * 1024, ((i) >> 3) * (4 << 20) + (((i) >> 2) & 1) * 4096)
#define ISSUE_V(lo, hi) do { _Pragma("unroll") for (int i_ = (lo); i_ < (hi); i_ += 2) { PACE2(44 + i_); V[i_] = LDV(i_); V[i_ + 1] = LDV(i_ + 1); } SB(); } while (0)
  v4u K[32], V[32], wqv[4], gv[4], uv[4];
#define LDWQ(t) wqv[t] = LDNT(rwq, g4 * 32768 + c * 16, (t) * 8192)
  LDWQ(0); K[0] = LDK(0); SB(); LDWQ(1); K[1] = LDK(1); SB(); LDWQ(2); K[2] = LDK(2); SB(); LDWQ(3); K[3] = LDK(3); SB();
  if (tid < 16) L.cnt[tid] = 0u;
  SB();
  BARRIER();
  const int cbg = ffn ? ((b >> 3) ^ 1) : 0, cbu = ffn ? ((b >> 3) ^ 2) : 0, fsz = ffn ? 15 * FF * 2 + 256 : 0;
  const __amdgpu_buffer_rsrc_t rg = MKRS(wg + (size_t)row0 * FF + 128 * cbg, fsz), ru = MKRS(wu + (size_t)row0 * FF + 128 * cbu, fsz);
#define LDG(t) gv[t] = LDNT(rg, g4 * (4 * FF * 2) + c * 16, (t) * (FF * 2))
#define LDU(t) uv[t] = LDNT(ru, g4 * (4 * FF * 2) + c * 16, (t) * (FF * 2))
#define KK(n, i) do { PACE2(n); K[i] = LDK(i); K[(i) + 1] = LDK((i) + 1); SB(); } while (0)
#define GK(n, t, i) do { PACE2(n); LDG(t); K[i] = LDK(i); SB(); } while (0)
#define UK(n, t, i) do { PACE2(n); LDU(t); K[i] = LDK(i); SB(); } while (0)
  if (w == 0) {
    float v[16];
#pragma unroll
    for (int i = 0; i < 4; ++i) { v[2 * i] = bf_lo(xa[i]); v[2 * i + 1] = bf_hi(xa[i]); v[8 + 2 * i] = bf_lo(xb[i]); v[9 + 2 * i] = bf_hi(xb[i]); }
    float s = 0.f;
#pragma unroll
    for (int i = 0; i < 16; ++i) s += v[i];
    const float mean = wave_sum(s) * (1.0f / D);
    float q2 = 0.f;
#pragma unroll
    for (int i = 0; i < 16; ++i) { const float d = v[i] - mean; q2 = fmaf(d, d, q2); }
    const float rstd = 1.0f / sqrtf(wave_sum(q2) * (1.0f / D) + 1e-5f);
    if (lane == 0) { L.ln[0] = f2u(mean); L.ln[1] = f2u(rstd); L.ln[2] = epoch + 0x01010101u; }
    ARRIVE(9);
  }
  SB();
  KK(8, 4); KK(10, 6); GK(12, 0, 8);
  WAITN(9, 1u);
  float xn[4];
  unsigned tag;
  {
    const v4u ln = *(const v4u*)&L.ln[0];
    const float mean = u2f(ln[0]), rstd = u2f(ln[1]);
    tag = ln[2];
    xn[0] = (bf_lo(xr[0]) - mean) * rstd; xn[1] = (bf_hi(xr[0]) - mean) * rstd; xn[2] = (bf_lo(xr[1]) - mean) * rstd; xn[3] = (bf_hi(xr[1]) - mean) * rstd;
  }
  {
    float acc[8];
#pragma unroll
    for (int i = 0; i < 8; ++i) acc[i] = 0.f;
#pragma unroll
    for (int t = 0; t < 4; ++t) fma8(acc, xn[t], wqv[t]);
    *(f32x2*)&L.redq[w][8 * c + 2 * g4] = tr_red4(acc);
  }
  ARRIVE(0);
  if (w == 0) {
    WAITN(0, 8u);
    VMWAIT(6);
    if (lane < 32) {
      f32x4 s = {0.f, 0.f, 0.f, 0.f};
#pragma unroll
      for (int i = 0; i < 8; ++i) s += *(const f32x4*)&L.redq[i][4 * lane];
      ST_SC1(pack24(s, tag), WS_Q + (grp * 8 + kb) * 4096 + (split >> 3) * 512 + lane * 16);
    }
  }
  SB();
  KK(14, 9); UK(16, 0, 11); KK(18, 12);
  const __amdgpu_buffer_rsrc_t rq1 = MKRS(ws + WS_Q + grp * 32768 + split * 64, w == 2 ? 7 * 4096 + 64 : 0);
#define Q1LD() __builtin_amdgcn_raw_buffer_load_b128(rq1, ((lane >> 2) & 7) * 4096 + (lane & 3) * 16, 0, AUX_SC1)
  v4u q1 = Q1LD();
  SB();
  GK(20, 1, 14); KK(22, 15); UK(24, 1, 17);
  if (w == 2 && !__all(ok24(q1, tag))) q1 = Q1LD();
  SB();
  KK(26, 18); GK(28, 2, 20);
  if (w == 2) {
    POLL_WAIT(ok24(q1, tag), { q1 = Q1LD(); });
    float v[4];
#pragma unroll
    for (int j = 0; j < 4; ++j) { float t = val24(q1[j]); t += dpp<ROR4>(t); t += dpp<XOR8>(t); v[j] = x16_sum(t); }
    if (lane < 4) ST_SC1((v4u{cvtpk(v[0], v[1]), tag, cvtpk(v[2], v[3]), tag}), WS_Q2 + grp * 4096 + (split * 4 + lane) * 16);
  }
  SB();
  KK(30, 21); UK(32, 2, 23); KK(34, 24); GK(36, 3, 26);
  const int qoff = WS_Q2 + grp * 4096 + w * 512 + (lane & 31) * 16;
#define QOK (qa[1] == tag && qa[3] == tag)
  v4u qa = LD_SC1(qoff);
  SB();
  KK(38, 27); UK(40, 3, 29); KK(42, 30);
  if (__builtin_expect(!__all(QOK), 0)) qa = LD_SC1(qoff);
  SB();
  ISSUE_V(0, 4);
  if (ffn) {
    float ag[8], au[8];
#pragma unroll
    for (int i = 0; i < 8; ++i) { ag[i] = 0.f; au[i] = 0.f; }
#pragma unroll
    for (int t = 0; t < 4; ++t) { fma8(ag, xn[t], gv[t]); fma8(au, xn[t], uv[t]); }
    *(f32x2*)&L.red[w][8 * c + 2 * g4] = tr_red4(ag);
    *(f32x2*)&L.red[w][128 + 8 * c + 2 * g4] = tr_red4(au);
    ARRIVE(3);
    if (w == 1) {
      WAITN(3, 8u);
      f32x4 sg = {0.f, 0.f, 0.f, 0.f};
#pragma unroll
      for (int i = 0; i < 8; ++i) sg += *(const f32x4*)&L.red[i][4 * lane];
      const int cb = lane < 32 ? cbg : cbu;
      ST_SC1(pack24(sg, tag), WS_F + ((2 * cb + ((lane >> 4) & 1)) * 8 + kb) * 512 + (lane >> 5) * 256 + c * 16);
    }
  }
  SB();
  ISSUE_V(4, 6);
  POLL_WAIT(QOK, { qa = LD_SC1(qoff); });
  if (lane < 32) *(v2u*)&L.qb[w][2 * lane] = v2u{qa[0], qa[2]};
  ARRIVE(1);
  ISSUE_V(6, 8);
  WAITN(1, 8u);
  v4u Q[2];
  Q[0] = *(const v4u*)&L.qb[c & 7][16 * (c >> 3) + 4 * g4];
  Q[1] = *(const v4u*)&L.qb[c & 7][32 + 16 * (c >> 3) + 4 * g4];
  constexpr float C2 = 0.08838834764831845f * 1.4426950408889634f;
  float S[8][4];
  float m = -INFINITY;
#pragma unroll
  for (int p = 0; p < 8; ++p) {
    f32x4 X = {0.f, 0.f, 0.f, 0.f}, Y = {0.f, 0.f, 0.f, 0.f};
    X = mfma(K[4 * p], Q[0], X); Y = mfma(K[4 * p + 2], Q[0], Y);
    X = mfma(K[4 * p + 1], Q[1], X); Y = mfma(K[4 * p + 3], Q[1], Y);
#pragma unroll
    for (int i = 0; i < 4; ++i) {
      auto sw = __builtin_amdgcn_permlane32_swap(f2u(X[i]), f2u(Y[i]), false, false);
      const float s = u2f(sw[0]) + dpp<XOR8>(u2f(sw[1]));
      S[p][i] = s; m = fmaxf(m, s);
    }
  }
  m = x16_max(m); m = x32_max(m);
  if (lane < 8) L.msm[lane][w] = m;
  ARRIVE(6);
  ISSUE_V(8, 14);
  WAITN(6, 8u);
  float mc;
  {
    const f32x4 m0 = *(const f32x4*)&L.msm[c & 7][0], m1 = *(const f32x4*)&L.msm[c & 7][4];
    mc = fmaxf(fmaxf(fmaxf(m0[0], m0[1]), fmaxf(m0[2], m0[3])), fmaxf(fmaxf(m1[0], m1[1]), fmaxf(m1[2], m1[3]))) * C2;
  }
  float l = 0.f;
  v4u P[4];
#pragma unroll
  for (int u = 0; u < 4; ++u)
#pragma unroll
    for (int jj = 0; jj < 4; ++jj) {
      const int p = 2 * u + (jj >> 1), i0 = 2 * (jj & 1);
      const float p0 = __builtin_amdgcn_exp2f(fmaf(S[p][i0], C2, -mc)), p1 = __builtin_amdgcn_exp2f(fmaf(S[p][i0 + 1], C2, -mc));
      const unsigned pk = cvtpk(p0, p1);
      l += bf_lo(pk) + bf_hi(pk);
      P[u][jj] = pk;
    }
  l = x16_sum(l); l = x32_sum(l);
  if (lane < 8) L.lsm[lane][w] = l;
  ARRIVE(4);
  f32x4 O[8];
#pragma unroll
  for (int i = 0; i < 8; ++i) O[i] = f32x4{0.f, 0.f, 0.f, 0.f};
#define PV(u) do { _Pragma("unroll") for (int i = 0; i < 8; ++i) { v4u bfrag; \
      _Pragma("unroll") for (int jj = 0; jj < 4; ++jj) \
        bfrag[jj] = __builtin_amdgcn_perm(V[8 * (u) + 2 * jj + 1][i >> 1], V[8 * (u) + 2 * jj][i >> 1], (i & 1) ? 0x07060302u : 0x05040100u); \
      O[i] = mfma(P[u], bfrag, O[i]); } SB(); } while (0)
  SB();
  PV(0);
  ISSUE_V(14, 18);
  if (w == 3) {
    WAITN(4, 8u);
    if (lane < 8) {
      const f32x4 l0 = *(const f32x4*)&L.lsm[lane][0], l1 = *(const f32x4*)&L.lsm[lane][4];
      const float Ls = ((l0[0] + l0[1]) + (l0[2] + l0[3])) + ((l1[0] + l1[1]) + (l1[2] + l1[3]));
      __builtin_amdgcn_raw_buffer_store_b64(v2u{f2u(mc + __builtin_amdgcn_logf(Ls)), tag}, rs, WS_ML + ((grp * 8 + lane) * 64 + split) * 8, 0, AUX_SC1);
    }
  }
  SB();
  const int rbd = (b >> 5) * 8 + (b & 7);
  const __amdgpu_buffer_rsrc_t rf = MKRS(ws + WS_F + (ffn ? rbd * 4096 : 0) + w * 512, ffn ? 512 : 0);
  const unsigned ftag = ffn ? tag : 0u;
#define FLD() __builtin_amdgcn_raw_buffer_load_b128(rf, (lane & 31) * 16, 0, AUX_SC1)
  v4u fv = FLD();
  ISSUE_V(18, 22);
  RESYNC();
  PV(1);
  ISSUE_V(22, 26);
  POLL_WAIT(ok24(fv, ftag), { fv = FLD(); });
  if (lane < 32) *(f32x4*)&L.fsl[w][4 * lane] = f32x4{val24(fv[0]), val24(fv[1]), val24(fv[2]), val24(fv[3])};
  ARRIVE(5);
  RESYNC();
  const int moff = WS_ML + ((grp * 8 + w) * 64 + lane) * 8;
#define MLD() __builtin_amdgcn_raw_buffer_load_b64(rs, moff, 0, AUX_SC1)
  v2u mv = MLD();
  ISSUE_V(26, 30);
  PV(2);
  if (w == 2) {
    WAITN(5, 8u);
    float gs = 0.f, us = 0.f;
#pragma unroll
    for (int i = 0; i < 8; ++i) { gs += L.fsl[i][lane]; us += L.fsl[i][64 + lane]; }
    L.hv[lane] = gelu_tanh(gs) * us;
  }
  SB();
  ISSUE_V(30, 32);
  const int cq4 = w & 3;
  const int wvo = r8 * (8 * 2048) + l8 * 16;
  v4u wv[8];
#define ML_FINISH() do { \
  POLL_WAIT(mv[1] == tag, { mv = MLD(); }); \
  { const float ms = u2f(mv[0]); const float M = wave_max(ms); \
    const float Lsum = wave_sum(__builtin_amdgcn_exp2f(ms - M)); \
    if (lane == 0) L.wsc[w] = __builtin_amdgcn_exp2f(u2f(__builtin_amdgcn_readlane(f2u(mc), w)) - M) * __builtin_amdgcn_rcpf(Lsum); } SB(); } while (0)
#define PV3() do { v4u bf_[8]; \
    _Pragma("unroll") for (int i = 0; i < 8; ++i) { _Pragma("unroll") for (int jj = 0; jj < 3; ++jj) \
        bf_[i][jj] = __builtin_amdgcn_perm(V[24 + 2 * jj + 1][i >> 1], V[24 + 2 * jj][i >> 1], (i & 1) ? 0x07060302u : 0x05040100u); } \
    SB(); \
    _Pragma("unroll") for (int i = 0; i < 8; ++i) { \
        bf_[i][3] = __builtin_amdgcn_perm(V[31][i >> 1], V[30][i >> 1], (i & 1) ? 0x07060302u : 0x05040100u); \
        O[i] = mfma(P[3], bf_[i], O[i]); } SB(); } while (0)
#define FINISH_ATTN() do { \
  PV3(); \
  if (g4 < 2) { _Pragma("unroll") for (int i = 0; i < 8; ++i) *(f32x4*)&L.osm[w][g4][i][c][0] = O[i]; } \
  BARRIER(); \
  { const float* src = &L.osm[0][w >> 2][2 * (w & 3)][0][0] + lane; \
    const float sc = L.wsc[4 * (w >> 2) + (lane & 3)]; \
    float o0 = 0.f, o1 = 0.f; \
    _Pragma("unroll") for (int i = 0; i < 8; ++i) { o0 += src[i * 1024]; o1 += src[i * 1024 + 64]; } \
    unsigned pk = cvtpk(o0 * sc, o1 * sc); \
    pk = pk < 0xFFFFFFFEu ? pk : 0xFFFFFFFEu;     \
    const v4u g = {pk, (unsigned)__builtin_amdgcn_mov_dpp((int)pk, 0x55, 0xf, 0xf, true), (unsigned)__builtin_amdgcn_mov_dpp((int)pk, 0xAA, 0xf, 0xf, true), \
                   (unsigned)__builtin_amdgcn_mov_dpp((int)pk, 0xFF, 0xf, 0xf, true)}; \
    if ((lane & 3) == 0) ST_SC1(g, WS_A + b * A_STR + w * 256 + lane * 4); } \
  SB(); } while (0)
  if (w < 4) {
    const __amdgpu_buffer_rsrc_t rwd = MKRS(wd + (size_t)(ffn ? 64 * rbd : 0) * D + 256 * cq + 64 * cq4, ffn ? 63 * 2048 + 128 : 0);
    ML_FINISH();
    TGATE(76);
#pragma unroll
    for (int t = 0; t < 4; ++t) wv[t] = LDNT(rwd, wvo + (t & 1) * 2048, (t >> 1) * 4096);
    SB();
    FINISH_ATTN();
#pragma unroll
    for (int t = 4; t < 8; ++t) wv[t] = LDNT(rwd, wvo + (t & 1) * 2048, (t >> 1) * 4096);
    SB();
    {
      float acc[8];
#pragma unroll
      for (int i = 0; i < 8; ++i) acc[i] = 0.f;
      if (ffn) {
        const f32x4 h0 = *(const f32x4*)&L.hv[8 * r8], h1 = *(const f32x4*)&L.hv[8 * r8 + 4];
#pragma unroll
        for (int t = 0; t < 4; ++t) { fma8(acc, h0[t], wv[t]); fma8(acc, h1[t], wv[4 + t]); }
      }
      const f32x4 zd = red8to4(acc);
      if ((lane & 24) == 0) *(f32x4*)&L.zx[cq4][8 * l8 + 4 * (lane >> 5)] = zd;
    }
    SB();
    const int aoff = WS_A + (grp * 64 + 16 * w + r8) * A_STR + tau * 128 + l8 * 16;
#define AOK (max(max(max(av[0], av[1]), max(av[2], av[3])), max(max(aw[0], aw[1]), max(aw[2], aw[3]))) != ~0u)
    v4u av = LD_SC1(aoff), aw = LD_SC1(aoff + 8 * A_STR);
    POLL_WAIT(AOK, { av = LD_SC1(aoff); aw = LD_SC1(aoff + 8 * A_STR); });
    {
      const float a[8] = {bf_lo(av[0]) + bf_lo(aw[0]), bf_hi(av[0]) + bf_hi(aw[0]), bf_lo(av[1]) + bf_lo(aw[1]), bf_hi(av[1]) + bf_hi(aw[1]),
                          bf_lo(av[2]) + bf_lo(aw[2]), bf_hi(av[2]) + bf_hi(aw[2]), bf_lo(av[3]) + bf_lo(aw[3]), bf_hi(av[3]) + bf_hi(aw[3])};
      L.cmb[w][8 * l8 + r8] = tr_red8(a, bit3);
    }
    ARRIVE(7);
    if (w == 0) {
      WAITN(8, 4u);
      { const unsigned td = NOW(); while ((int)(NOW() - td) < Z_DELAY) { } }
      const int zoff = WS_Z + ((r8 << 5) | (cq << 3) | l8) * Z_STR + piece * 16;
      v4u za = LD_SC1(zoff);
      POLL_WAIT(ok24(za, tag), { za = LD_SC1(zoff); });
      float u = tr16(tr32(val24(za[0]), val24(za[2])), tr32(val24(za[1]), val24(za[3])));
      u += dpp<XOR1>(u); u += dpp<XOR2>(u); u += dpp<XOR7>(u); u += dpp<XOR8>(u);
      if (c == 0) out[ocol + g4] = (unsigned short)cvtpk(bf_lo(xres) + u, 0.f);
      if (b == 0 && lane == 0) __builtin_amdgcn_raw_buffer_store_b32(tag, rs, WS_EPOCH, 0, AUX_SC1);
    }
  } else {
    const __amdgpu_buffer_rsrc_t rwo = MKRS(wo + (size_t)((8 * grp + 4 * (tau >> 3)) * 128 + 64 * (tau & 1) + 2 * ((tau >> 1) & 3)) * D + 256 * cq + 64 * cq4, 441 * 2048 + 128);
    ML_FINISH();
    FINISH_ATTN();
#pragma unroll
    for (int t = 0; t < 8; ++t) wv[t] = LDNT(rwo, wvo + (t & 1) * 2048, (t >> 1) * (128 * 2048));
    SB();
    f32x2 wf[8][4];
#pragma unroll
    for (int t = 0; t < 8; ++t)
#pragma unroll
      for (int d = 0; d < 4; ++d) { wf[t][d] = f32x2{bf_lo(wv[t][d]), bf_hi(wv[t][d])}; asm volatile("" : "+v"(wf[t][d])); }
    SB();
    WAITN(7, 4u);
    const f32x4 ov = *(const f32x4*)&L.cmb[l8 & 3][8 * r8 + (l8 & 4)];
    const f32x4 zd = *(const f32x4*)&L.zx[cq4][8 * l8 + 4 * (lane >> 5)];
    float oa[4], ob[4];
#pragma unroll
    for (int t = 0; t < 4; ++t) { float v = ov[t]; v += dpp<XOR1>(v); v += dpp<XOR2>(v); const float o = dpp<XOR7>(v); oa[t] = (l8 & 4) ? o : v; ob[t] = (l8 & 4) ? v : o; }
    f32x2 ac2[4] = {{0.f, 0.f}, {0.f, 0.f}, {0.f, 0.f}, {0.f, 0.f}};
#pragma unroll
    for (int t = 0; t < 4; ++t)
#pragma unroll
      for (int d = 0; d < 4; ++d) {
        ac2[d] = __builtin_elementwise_fma(wf[t][d], f32x2{oa[t], oa[t]}, ac2[d]);
        ac2[d] = __builtin_elementwise_fma(wf[4 + t][d], f32x2{ob[t], ob[t]}, ac2[d]);
      }
    const float acc[8] = {ac2[0][0], ac2[0][1], ac2[1][0], ac2[1][1], ac2[2][0], ac2[2][1], ac2[3][0], ac2[3][1]};
    const f32x4 z = red8to4(acc) + zd;
    if ((lane & 24) == 0) ST_SC1(pack24(z, tag), WS_Z + b * Z_STR + (64 * cq4 + 8 * l8 + 4 * (lane >> 5)) * 4);
    ARRIVE(8);
  }
}

extern "C" void kernel_launch(void* const* d_in, const int* in_sizes, int n_in, void* d_out, int out_size, void* d_ws, size_t ws_size,
                              hipStream_t stream) {
  fused_decode_kernel<<<NWG, NTHR, 0, stream>>>((const unsigned short*)d_in[0], (const unsigned short*)d_in[1], (const unsigned short*)d_in[2],
                                                (const unsigned short*)d_in[3], (const unsigned short*)d_in[4], (const unsigned short*)d_in[6],
                                                (const unsigned short*)d_in[7], (const unsigned short*)d_in[8], (unsigned char*)d_ws,
                                                (unsigned short*)d_out);
}
```
